```python
import torch

def _softmax(x, axis=-1):
    return torch.softmax(x, dim=axis)
def _gelu(x):
    return torch.nn.functional.gelu(x, approximate="tanh")
def _silu(x):
    return torch.nn.functional.silu(x)
def _sigmoid(x):
    return torch.sigmoid(x)
def _layernorm(x, axis=-1, eps=1e-5):
    m = x.mean(dim=axis, keepdim=True)
    v = x.var(dim=axis, keepdim=True, unbiased=False)
    return (x - m) / torch.sqrt(v + eps)
def _scatter_add(tgt, idx, src, axis=0):
    return tgt.clone().index_add_(axis, idx, src)
def _segment_sum(src, seg, n):
    out = torch.zeros((n,) + src.shape[1:], dtype=src.dtype)
    return out.index_add_(0, seg, src)
def _pad(x, pw):
    flat = tuple(v for pair in reversed(pw) for v in pair)
    return torch.nn.functional.pad(x, flat)

kernel_name = 'standalone__structured_sparse_n_m__ragged'

def setup_inputs():

    _w = (lambda a: (lambda t: t.to(torch.bfloat16) if t.is_floating_point() else t)(torch.as_tensor(a)))
    B, S, D = 4, 4096, 1024
    assert D % 4 == 0, "d_model must be divisible by M=4 for 2:4 sparsity"
    return {
        "x": _w((lambda *s: torch.randn(*s, dtype=torch.bfloat16) * 0.05)(B, S, D)),
        "w": _w((lambda *s: torch.randn(*s, dtype=torch.bfloat16) * 0.05)(D, D)),
        "b": _w((lambda *s: torch.randn(*s, dtype=torch.bfloat16) * 0.05)(D)),
    }

def reference(x, w, b):
    B, S, D = x.shape
    N, M = 2, 4
    G = D // M
    wg = w.reshape(G, M, D)
    mag = torch.abs(wg)
    order = torch.argsort(mag, 1)
    thr = (lambda x, i, axis: torch.take_along_dim(x, i, dim=axis))(mag, order[:, M - N:M - N + 1, :], 1)
    mask = (lambda x, dt: x.to(dt))(mag >= thr, w.dtype)
    wp = (wg * mask).reshape(D, D)
    return torch.einsum("bsd,df->bsf", x, wp) + b
```

```cpp
#include <hip/hip_runtime.h>
#include <hip/hip_bf16.h>
#include <stdint.h>

typedef uint32_t u32x4 __attribute__((ext_vector_type(4)));
typedef uint32_t u32x2 __attribute__((ext_vector_type(2)));

__global__ __launch_bounds__(256, 1) void sp24_fused(const void* x, void* out, unsigned char* ws, const void* bias, const unsigned short* w) {
  __shared__ __attribute__((aligned(16))) char lds[163840];
  const int tid = threadIdx.x;
  const int wave = __builtin_amdgcn_readfirstlane(tid >> 6);
  uint32_t tag;
  {
    const int wg = blockIdx.x >> 4, ks = blockIdx.x & 15;
    const int l = tid & 63, r = l & 15, tt = (l >> 4) & 1, h = wave >> 1, qa = (wave & 1) * 2 + (l >> 5), t = 2 * h + tt;
    const int f = wg * 64 + 32 * h + 8 * (r >> 2) + 4 * tt + (r & 3);
    const uint32_t woff = (uint32_t)((64 * ks + 16 * qa + 2) * 1024 + f) * 2u;
    uint32_t raw[16], ep, d0, d1, d2, d3;
    const uint32_t zero = 0;
    asm volatile(
        "s_getpc_b64 s[26:27]\n\t"
        "s_mul_i32 s17, %30, 40960\n\t"
        "s_lshl_b32 s28, %30, 13\n\t"
        "s_add_u32 m0, s17, 39936\n\t"
        "v_lshl_add_u32 %17, %31, 7, s28\n\t"
        "global_load_lds_dword %17, s[26:27]\n\t"
        "global_load_dword %16, %25, %26 sc1\n\t"
        "global_load_ushort %0, %21, %27 offset:-4096\n\tglobal_load_ushort %1, %21, %27 offset:-2048\n\tglobal_load_ushort %2, %21, %27\n\tglobal_load_ushort %3, %21, %27 offset:2048\n\t"
        "global_load_ushort %4, %22, %27 offset:-4096\n\tglobal_load_ushort %5, %22, %27 offset:-2048\n\tglobal_load_ushort %6, %22, %27\n\tglobal_load_ushort %7, %22, %27 offset:2048\n\t"
        "global_load_ushort %8, %23, %27 offset:-4096\n\tglobal_load_ushort %9, %23, %27 offset:-2048\n\tglobal_load_ushort %10, %23, %27\n\tglobal_load_ushort %11, %23, %27 offset:2048\n\t"
        "global_load_ushort %12, %24, %27 offset:-4096\n\tglobal_load_ushort %13, %24, %27 offset:-2048\n\tglobal_load_ushort %14, %24, %27\n\tglobal_load_ushort %15, %24, %27 offset:2048\n\t"
        "s_lshr_b32 s27, %29, 3\n\t"
        "s_and_b32 s38, s27, 3\n\t"
        "s_and_b32 s26, %29, 7\n\t"
        "s_lshl_b32 s26, s26, 3\n\t"
        "s_lshr_b32 s28, s27, 2\n\t"
        "s_add_u32 s26, s26, s28\n\t"
        "s_lshl_b32 s42, s26, 19\n\t"
        "s_mov_b64 s[8:9], %28\n\t"
        "s_add_u32 s8, s8, s42\n\t"
        "s_addc_u32 s9, s9, 0\n\t"
        "s_and_b32 s9, s9, 0xffff\n\t"
        "s_mov_b32 s10, 0x80000\n\t"
        "s_mov_b32 s11, 0x20000\n\t"
        "s_lshl_b32 s43, %30, 2\n\t"
        "s_movk_i32 s41, 10240\n\t"
        "s_mul_i32 s17, s43, s41\n\t"
        "s_and_b32 s53, s38, 2\n\t"
        "s_lshl_b32 s53, s53, 17\n\t"
        "s_add_u32 s34, s43, 0\n\t"
        "v_xor_b32 %17, s34, %31\n\t"
        "s_lshl_b32 s34, s34, 11\n\t"
        "v_lshl_add_u32 %17, %17, 4, s34\n\t"
        "s_add_u32 s34, s43, 1\n\t"
        "v_xor_b32 %18, s34, %31\n\t"
        "s_lshl_b32 s34, s34, 11\n\t"
        "v_lshl_add_u32 %18, %18, 4, s34\n\t"
        "s_add_u32 s34, s43, 2\n\t"
        "v_xor_b32 %19, s34, %31\n\t"
        "s_lshl_b32 s34, s34, 11\n\t"
        "v_lshl_add_u32 %19, %19, 4, s34\n\t"
        "s_add_u32 s34, s43, 3\n\t"
        "v_xor_b32 %20, s34, %31\n\t"
        "s_lshl_b32 s34, s34, 11\n\t"
        "v_lshl_add_u32 %20, %20, 4, s34\n\t"
        "s_add_u32 m0, s17, 0\n\t"
        "s_add_u32 s40, s53, 0\n\t"
        "buffer_load_dwordx4 %17, s[8:11], s40 offen offset:0  lds\n\t"
        "s_add_u32 m0, s17, 10240\n\t"
        "s_add_u32 s40, s53, 0\n\t"
        "buffer_load_dwordx4 %18, s[8:11], s40 offen offset:0  lds\n\t"
        "s_add_u32 m0, s17, 20480\n\t"
        "s_add_u32 s40, s53, 0\n\t"
        "buffer_load_dwordx4 %19, s[8:11], s40 offen offset:0  lds\n\t"
        "s_add_u32 m0, s17, 30720\n\t"
        "s_add_u32 s40, s53, 0\n\t"
        "buffer_load_dwordx4 %20, s[8:11], s40 offen offset:0  lds\n\t"
        "s_add_u32 m0, s17, 2048\n\t"
        "s_add_u32 s40, s53, 32768\n\t"
        "buffer_load_dwordx4 %17, s[8:11], s40 offen offset:0  lds\n\t"
        "s_add_u32 m0, s17, 12288\n\t"
        "s_add_u32 s40, s53, 32768\n\t"
        "buffer_load_dwordx4 %18, s[8:11], s40 offen offset:0  lds\n\t"
        "s_add_u32 m0, s17, 22528\n\t"
        "s_add_u32 s40, s53, 32768\n\t"
        "buffer_load_dwordx4 %19, s[8:11], s40 offen offset:0  lds\n\t"
        "s_add_u32 m0, s17, 32768\n\t"
        "s_add_u32 s40, s53, 32768\n\t"
        "buffer_load_dwordx4 %20, s[8:11], s40 offen offset:0  lds\n\t"
        "s_waitcnt vmcnt(8)"
        : "=&v"(raw[0]), "=&v"(raw[1]), "=&v"(raw[2]), "=&v"(raw[3]), "=&v"(raw[4]), "=&v"(raw[5]), "=&v"(raw[6]), "=&v"(raw[7]),
          "=&v"(raw[8]), "=&v"(raw[9]), "=&v"(raw[10]), "=&v"(raw[11]), "=&v"(raw[12]), "=&v"(raw[13]), "=&v"(raw[14]), "=&v"(raw[15]),
          "=&v"(ep), "=&v"(d0), "=&v"(d1), "=&v"(d2), "=&v"(d3)
        : "v"(woff), "v"(woff + 8192u), "v"(woff + 16384u), "v"(woff + 24576u), "v"(zero), "s"(ws + 0x1001000), "s"(w),
          "s"(x), "s"(blockIdx.x), "s"(wave), "v"(l)
        : "memory", "scc", "s8", "s9", "s10", "s11", "s17", "s26", "s27", "s28", "s34", "s38", "s40", "s41", "s42", "s43", "s53");
    tag = __builtin_amdgcn_readfirstlane(ep) + 1u;
    uint32_t a[4], e[4], idx = 0, kmax = 0;
#pragma unroll
    for (int j = 0; j < 4; j++) {
      const uint32_t k0 = raw[4 * j] & 0x7fffu, k1 = raw[4 * j + 1] & 0x7fffu, k2 = raw[4 * j + 2] & 0x7fffu, k3 = raw[4 * j + 3] & 0x7fffu;
      const uint32_t hi1 = max(k0, k1), hi2 = max(k2, k3);
      const uint32_t thr = max(max(min(hi1, hi2), max(min(k0, k1), min(k2, k3))), 1u);
      kmax = max(kmax, max(hi1, hi2));
      const uint32_t m = (k0 >= thr ? 1u : 0u) | (k1 >= thr ? 2u : 0u) | (k2 >= thr ? 4u : 0u) | (k3 >= thr ? 8u : 0u);
      const uint32_t ii = (uint32_t)(0x498e4dc749864144ull >> (4 * m)) & 15u;
      const uint32_t p01 = raw[4 * j] | (raw[4 * j + 1] << 16), p23 = raw[4 * j + 2] | (raw[4 * j + 3] << 16);
      a[j] = __builtin_amdgcn_perm(p23, p01, 0x01000100u + (ii & 3u) * 0x0202u + (ii >> 2) * 0x02020000u);
      const uint32_t eb = (0xe8804000u >> (2 * m)) & 3u;
      e[j] = p23 & (((eb & 1u) ? 0xffffu : 0u) | ((eb & 2u) ? 0xffff0000u : 0u));
      idx |= ii << (4 * j);
    }
    if (__builtin_expect(__any(kmax >= 0x7f80u), 0)) {
      idx = 0;
#pragma unroll 1
      for (int j = 0; j < 4; j++) {
        uint32_t v[4], wp[4];
#pragma unroll
        for (int i = 0; i < 4; i++) v[i] = j == 0 ? raw[i] : j == 1 ? raw[4 + i] : j == 2 ? raw[8 + i] : raw[12 + i];
        const uint32_t k0 = v[0] & 0x7fffu, k1 = v[1] & 0x7fffu, k2 = v[2] & 0x7fffu, k3 = v[3] & 0x7fffu;
        const uint32_t s2 = max(min(max(k0, k1), max(k2, k3)), max(min(k0, k1), min(k2, k3)));
        uint32_t m = 0;
#pragma unroll
        for (int i = 0; i < 4; i++) {
          const uint32_t k = v[i] & 0x7fffu;
          const bool keep = s2 <= 0x7f80u && k <= 0x7f80u && k >= s2;
          wp[i] = keep ? (k ? v[i] : 0u) : (k >= 0x7f80u ? 0x7fc0u : 0u);
          m |= wp[i] ? 1u << i : 0u;
        }
        const uint32_t ii = (uint32_t)(0x498e4dc749864144ull >> (4 * m)) & 15u;
        const uint32_t p01 = wp[0] | (wp[1] << 16), p23 = wp[2] | (wp[3] << 16);
        const uint32_t av = __builtin_amdgcn_perm(p23, p01, 0x01000100u + (ii & 3u) * 0x0202u + (ii >> 2) * 0x02020000u);
        const uint32_t eb = (0xe8804000u >> (2 * m)) & 3u;
        const uint32_t ev = p23 & (((eb & 1u) ? 0xffffu : 0u) | ((eb & 2u) ? 0xffff0000u : 0u));
#pragma unroll
        for (int jj = 0; jj < 4; jj++) { a[jj] = jj == j ? av : a[jj]; e[jj] = jj == j ? ev : e[jj]; }
        idx |= ii << (4 * j);
      }
    }
    unsigned char* base = ws + (size_t)wg * 0x80000u;
    const uint32_t fl = qa * 16 + r;
    {
      const u32x4 av = {a[0], a[1], a[2], a[3]}, ev = {e[0], e[1], e[2], e[3]};
      const uint32_t o = (ks * 4 + t) * 1024 + fl * 16;
      const uint32_t other = __builtin_amdgcn_ds_swizzle(idx, 0x401f);
      const uint32_t iw = tt ? (other | (idx << 16)) : (idx | (other << 16));
      const uint32_t oi = 0x10000u + (ks >> 1) * 1024 + fl * 16 + (ks & 1) * 8 + h * 4;
      asm volatile("global_store_dwordx4 %0, %1, %6 sc1\n\tglobal_store_dword %4, %5, %6 sc1\n\tglobal_store_dwordx4 %2, %3, %6 sc1\n\ts_nop 1"
                   :: "v"(o), "v"(av), "v"(o + 0x20000u), "v"(ev), "v"(oi), "v"(iw), "s"(base) : "memory");
    }
    __builtin_amdgcn_sched_barrier(0);
    uint32_t mq = 0;
#pragma unroll
    for (int j = 0; j < 4; j++) {
      const unsigned long long bal = __ballot(e[j] != 0u);
#pragma unroll
      for (int q = 0; q < 4; q++) mq |= (((bal >> (16 * q)) & 0xffffull) ? 1u : 0u) << (4 * q + j);
    }
    if (mq) {
#pragma unroll
      for (int j = 0; j < 4; j++)
        if ((mq >> (4 * (l >> 4) + j)) & 1u) {
          const uint32_t o = 0x40000u + t * 0x4400u + ((ks * 16 + qa * 4 + j) * 16 + r) * 4u;
          asm volatile("global_store_dword %0, %1, %2 sc1" :: "v"(o), "v"(e[j]), "s"(base) : "memory");
        }
    }
    if (ks == 0 && tid < 16) {
      const uint32_t o = 0x40000u + (256 * 16 + tid) * 4u;
      asm volatile("global_store_dword %0, %1, %2 sc1" :: "v"(o), "v"(zero), "s"(base) : "memory");
    }
    unsigned char* sm = (unsigned char*)(lds + 8192);
    if (l < 2) sm[(2 * h + l) * 2 + (wave & 1)] = (unsigned char)(((mq >> (4 * l)) & 15u) | (((mq >> (8 + 4 * l)) & 15u) << 4));
    asm volatile(

        "s_waitcnt vmcnt(0) lgkmcnt(0)"
        : "=&v"(d0), "=&v"(d1), "=&v"(d2), "=&v"(d3) : "s"(x), "s"(blockIdx.x), "s"(wave), "v"(l)
        : "memory", "scc", "s8", "s9", "s10", "s11", "s17", "s26", "s27", "s28", "s34", "s38", "s40", "s41", "s42", "s43", "s53");
    __builtin_amdgcn_s_barrier();
    asm volatile("" ::: "memory");
    if (tid < 2) {
      const u32x2 g = {*(const uint32_t*)(sm + tid * 4), tag};
      const uint32_t o = blockIdx.x * 16 + tid * 8;
      asm volatile("global_store_dwordx2 %0, %1, %2 sc1" :: "v"(o), "v"(g), "s"(ws + 0x1000000) : "memory");
    }
  }
  asm volatile(
    "v_mbcnt_lo_u32_b32 v69, -1, 0\n\t"
    "v_mbcnt_hi_u32_b32 v69, -1, v69\n\t"
    "s_lshr_b32 s27, %3, 3\n\t"
    "s_and_b32 s38, s27, 3\n\t"
    "s_lshl_b32 s29, s38, 2\n\t"
    "s_add_u32 s29, s29, %4\n\t"
    "s_lshl_b32 s40, s29, 19\n\t"
    "s_mov_b64 s[30:31], %2\n\t"
    "s_add_u32 s14, s30, s40\n\t"
    "s_addc_u32 s15, s31, 0\n\t"
    "s_add_u32 s36, s14, 69632\n\t"
    "s_addc_u32 s37, s15, 0\n\t"
    "s_and_b32 s26, %3, 7\n\t"
    "s_lshl_b32 s26, s26, 3\n\t"
    "s_lshr_b32 s28, s27, 2\n\t"
    "s_add_u32 s26, s26, s28\n\t"
    "v_and_b32 v70, 15, v69\n\t"
    "v_lshrrev_b32 v71, 4, v69\n\t"
    "s_lshl_b32 s42, s26, 19\n\t"
    "s_mov_b64 s[30:31], %0\n\t"
    "s_add_u32 s8, s30, s42\n\t"
    "s_addc_u32 s9, s31, 0\n\t"
    "s_and_b32 s9, s9, 0xffff\n\t"
    "s_mov_b32 s10, 0x80000\n\t"
    "s_mov_b32 s11, 0x20000\n\t"
    "s_lshl_b32 s43, %4, 2\n\t"
    "s_movk_i32 s41, 10240\n\t"
    "s_mul_i32 s17, s43, s41\n\t"
    "s_add_u32 s34, s43, 0\n\t"
    "v_xor_b32 v6, s34, v70\n\t"
    "v_lshlrev_b32 v6, 4, v6\n\t"
    "v_lshl_add_u32 v6, v71, 8, v6\n\t"
    "s_lshl_b32 s34, s34, 11\n\t"
    "v_add_u32 v6, s34, v6\n\t"
    "s_add_u32 s34, s43, 1\n\t"
    "v_xor_b32 v7, s34, v70\n\t"
    "v_lshlrev_b32 v7, 4, v7\n\t"
    "v_lshl_add_u32 v7, v71, 8, v7\n\t"
    "s_lshl_b32 s34, s34, 11\n\t"
    "v_add_u32 v7, s34, v7\n\t"
    "s_add_u32 s34, s43, 2\n\t"
    "v_xor_b32 v8, s34, v70\n\t"
    "v_lshlrev_b32 v8, 4, v8\n\t"
    "v_lshl_add_u32 v8, v71, 8, v8\n\t"
    "s_lshl_b32 s34, s34, 11\n\t"
    "v_add_u32 v8, s34, v8\n\t"
    "s_add_u32 s34, s43, 3\n\t"
    "v_xor_b32 v9, s34, v70\n\t"
    "v_lshlrev_b32 v9, 4, v9\n\t"
    "v_lshl_add_u32 v9, v71, 8, v9\n\t"
    "s_lshl_b32 s34, s34, 11\n\t"
    "v_add_u32 v9, s34, v9\n\t"
    "s_and_b32 s53, s38, 2\n\t"
    "s_lshl_b32 s53, s53, 17\n\t"
    "s_mov_b32 s52, 131072\n\t"
    "s_xor_b32 s16, s52, s53\n\t"
    "s_mov_b32 s54, 0\n\t"
    "s_mov_b32 s55, s53\n\t"
    "v_lshlrev_b32 v11, 4, v69\n\t"
    "s_lshl_b32 s39, s29, 7\n\t"
    "s_mov_b64 s[44:45], %5\n\t"
    "s_add_u32 s44, s44, s39\n\t"
    "s_addc_u32 s45, s45, 0\n\t"
    "v_lshlrev_b32 v68, 4, v71\n\t"
    "global_load_dwordx4 v[60:63], v68, s[44:45] offset:0\n\t"
    "global_load_dwordx4 v[64:67], v68, s[44:45] offset:64\n\t"
    "s_mov_b64 s[30:31], s[14:15]\n\t"
    "s_mov_b64 s[44:45], %1\n\t"
    "s_add_u32 s12, s44, s42\n\t"
    "s_addc_u32 s13, s45, 0\n\t"
    "s_add_u32 s48, s12, s39\n\t"
    "s_addc_u32 s49, s13, 0\n\t"
    "s_and_b32 s49, s49, 0xffff\n\t"
    "s_sub_u32 s50, 0x80000, s39\n\t"
    "s_mov_b32 s51, 0x20000\n\t"
    "s_add_u32 s20, s14, 131072\n\t"
    "s_addc_u32 s21, s15, 0\n\t"
    "v_and_b32 v0, 7, v70\n\t"
    "v_lshlrev_b32 v0, 11, v0\n\t"
    "v_lshl_add_u32 v0, v71, 4, v0\n\t"
    "v_lshrrev_b32 v1, 3, v70\n\t"
    "v_lshl_add_u32 v0, v1, 6, v0\n\t"
    "s_add_u32 m0, s17, 4096\n\t"
    "s_add_u32 s40, s53, 65536\n\t"
    "buffer_load_dwordx4 v6, s[8:11], s40 offen offset:0  lds\n\t"
    "v_add_u32 v1, 0x4000, v0\n\t"
    "v_mul_lo_u32 v252, v70, s41\n\t"
    "v_lshlrev_b32 v253, 4, v70\n\t"
    "v_add_u32 v2, 0, v71\n\t"
    "v_xor_b32 v2, v2, v70\n\t"
    "s_add_u32 m0, s17, 14336\n\t"
    "s_add_u32 s40, s53, 65536\n\t"
    "buffer_load_dwordx4 v7, s[8:11], s40 offen offset:0  lds\n\t"
    "v_lshl_add_u32 v2, v2, 4, v252\n\t"
    "v_add_u32 v3, 4, v71\n\t"
    "v_xor_b32 v3, v3, v70\n\t"
    "v_lshl_add_u32 v3, v3, 4, v252\n\t"
    "v_add_u32 v4, 8, v71\n\t"
    "s_add_u32 m0, s17, 24576\n\t"
    "s_add_u32 s40, s53, 65536\n\t"
    "buffer_load_dwordx4 v8, s[8:11], s40 offen offset:0  lds\n\t"
    "v_xor_b32 v4, v4, v70\n\t"
    "v_lshl_add_u32 v4, v4, 4, v252\n\t"
    "v_add_u32 v5, 12, v71\n\t"
    "v_xor_b32 v5, v5, v70\n\t"
    "v_lshl_add_u32 v5, v5, 4, v252\n\t"
    "s_add_u32 m0, s17, 34816\n\t"
    "s_add_u32 s40, s53, 65536\n\t"
    "buffer_load_dwordx4 v9, s[8:11], s40 offen offset:0  lds\n\t"
    "v_mov_b32 v10, 0xeeeeeeee\n\t"
    "s_add_u32 s34, s17, 8192\n\t"
    "v_and_b32 v76, 2, v69\n\t"
    "v_lshlrev_b32 v76, 18, v76\n\t"
    "v_or_b32 v76, 0x100, v76\n\t"
    "s_add_u32 m0, s17, 6144\n\t"
    "s_add_u32 s40, s53, 98304\n\t"
    "buffer_load_dwordx4 v6, s[8:11], s40 offen offset:0  lds\n\t"
    "v_lshl_add_u32 v77, v69, 2, s34\n\t"
    "ds_write_b32 v77, v76 offset:0\n\t"
    "ds_write_b32 v77, v76 offset:256\n\t"
    "ds_write_b32 v77, v76 offset:512\n\t"
    "v_mul_u32_u24 v75, 192, v71\n\t"
    "s_add_u32 m0, s17, 16384\n\t"
    "s_add_u32 s40, s53, 98304\n\t"
    "buffer_load_dwordx4 v7, s[8:11], s40 offen offset:0  lds\n\t"
    "v_add_u32 v75, s34, v75\n\t"
    "v_lshlrev_b32 v95, 4, v70\n\t"
    "v_mul_u32_u24 v96, 272, v71\n\t"
    "v_and_b32 v97, 3, v69\n\t"
    "v_lshlrev_b32 v97, 4, v97\n\t"
    "s_add_u32 m0, s17, 26624\n\t"
    "s_add_u32 s40, s53, 98304\n\t"
    "buffer_load_dwordx4 v8, s[8:11], s40 offen offset:0  lds\n\t"
    "v_lshlrev_b32 v254, 2, v70\n\t"
    "v_lshl_add_u32 v254, v71, 8, v254\n\t"
    "v_add_u32 v254, s34, v254\n\t"
    "v_add_u32 v255, s34, v11\n\t"
    "s_add_u32 s56, s14, 262144\n\t"
    "s_addc_u32 s57, s15, 0\n\t"
    "s_add_u32 m0, s17, 36864\n\t"
    "s_add_u32 s40, s53, 98304\n\t"
    "buffer_load_dwordx4 v9, s[8:11], s40 offen offset:0  lds\n\t"
    "s_waitcnt vmcnt(8)\n\t"
    "v_lshlrev_b32 v132, 16, v60\n\t"
    "v_and_b32 v133, 0xffff0000, v60\n\t"
    "v_lshlrev_b32 v134, 16, v61\n\t"
    "v_and_b32 v135, 0xffff0000, v61\n\t"
    "v_lshlrev_b32 v136, 16, v62\n\t"
    "v_and_b32 v137, 0xffff0000, v62\n\t"
    "v_lshlrev_b32 v138, 16, v63\n\t"
    "v_and_b32 v139, 0xffff0000, v63\n\t"
    "v_lshlrev_b32 v140, 16, v64\n\t"
    "v_and_b32 v141, 0xffff0000, v64\n\t"
    "v_lshlrev_b32 v142, 16, v65\n\t"
    "v_and_b32 v143, 0xffff0000, v65\n\t"
    "v_lshlrev_b32 v144, 16, v66\n\t"
    "v_and_b32 v145, 0xffff0000, v66\n\t"
    "v_lshlrev_b32 v146, 16, v67\n\t"
    "v_and_b32 v147, 0xffff0000, v67\n\t"
    "v_mov_b64 v[100:101], v[132:133]\n\t"
    "v_mov_b64 v[102:103], v[134:135]\n\t"
    "v_mov_b64 v[104:105], v[136:137]\n\t"
    "v_mov_b64 v[106:107], v[138:139]\n\t"
    "v_mov_b64 v[108:109], v[140:141]\n\t"
    "v_mov_b64 v[110:111], v[142:143]\n\t"
    "v_mov_b64 v[112:113], v[144:145]\n\t"
    "v_mov_b64 v[114:115], v[146:147]\n\t"
    "v_mov_b64 v[116:117], v[132:133]\n\t"
    "v_mov_b64 v[118:119], v[134:135]\n\t"
    "v_mov_b64 v[120:121], v[136:137]\n\t"
    "v_mov_b64 v[122:123], v[138:139]\n\t"
    "v_mov_b64 v[124:125], v[140:141]\n\t"
    "v_mov_b64 v[126:127], v[142:143]\n\t"
    "v_mov_b64 v[128:129], v[144:145]\n\t"
    "v_mov_b64 v[130:131], v[146:147]\n\t"
    "v_mov_b64 v[236:237], v[132:133]\n\t"
    "v_mov_b64 v[238:239], v[134:135]\n\t"
    "v_mov_b64 v[240:241], v[136:137]\n\t"
    "v_mov_b64 v[242:243], v[138:139]\n\t"
    "v_mov_b64 v[244:245], v[140:141]\n\t"
    "v_mov_b64 v[246:247], v[142:143]\n\t"
    "v_mov_b64 v[248:249], v[144:145]\n\t"
    "v_mov_b64 v[250:251], v[146:147]\n\t"
    "v_mov_b64 v[44:45], v[132:133]\n\t"
    "v_mov_b64 v[46:47], v[134:135]\n\t"
    "v_mov_b64 v[48:49], v[136:137]\n\t"
    "v_mov_b64 v[50:51], v[138:139]\n\t"
    "v_mov_b64 v[52:53], v[140:141]\n\t"
    "v_mov_b64 v[54:55], v[142:143]\n\t"
    "v_mov_b64 v[56:57], v[144:145]\n\t"
    "v_mov_b64 v[58:59], v[146:147]\n\t"
    "s_mov_b64 s[44:45], %2\n\t"
    "s_lshl_b32 s46, s29, 8\n\t"
    "s_add_u32 s46, s46, 0x1000000\n\t"
    "s_add_u32 s44, s44, s46\n\t"
    "s_addc_u32 s45, s45, 0\n\t"
    "v_lshrrev_b32 v68, 1, v71\n\t"
    "v_lshlrev_b32 v68, 3, v68\n\t"
    "v_lshl_add_u32 v68, v70, 4, v68\n\t"
    "s_mov_b32 s46, 0x10000\n\t"
    ".Lpoll_%=:\n\t"
    "global_load_dwordx2 v[72:73], v68, s[44:45] sc1\n\t"
    "s_waitcnt vmcnt(0)\n\t"
    "v_cmp_ne_u32 vcc, %6, v73\n\t"
    "s_cbranch_vccz .Lpolled_%=\n\t"
    "s_sleep 2\n\t"
    "s_sub_u32 s46, s46, 1\n\t"
    "s_cmp_lg_u32 s46, 0\n\t"
    "s_cbranch_scc1 .Lpoll_%=\n\t"
    ".Lpolled_%=:\n\t"
    "v_and_b32 v68, 1, v71\n\t"
    "v_lshlrev_b32 v68, 4, v68\n\t"
    "v_bfe_u32 v72, v72, v68, 16\n\t"
    "global_load_dwordx4 v[12:15], v11, s[36:37] offset:-4096 sc1\n\t"
    "global_load_dwordx4 v[16:19], v11, s[36:37] offset:-3072 sc1\n\t"
    "global_load_dwordx4 v[20:23], v11, s[36:37] offset:-2048 sc1\n\t"
    "global_load_dwordx4 v[24:27], v11, s[36:37] offset:-1024 sc1\n\t"
    "global_load_dwordx4 a[0:3], v11, s[30:31] offset:0 sc1\n\t"
    "global_load_dwordx4 a[4:7], v11, s[30:31] offset:1024 sc1\n\t"
    "global_load_dwordx4 a[8:11], v11, s[30:31] offset:2048 sc1\n\t"
    "global_load_dwordx4 a[12:15], v11, s[30:31] offset:3072 sc1\n\t"
    "s_add_u32 s30, s30, 4096\n\t"
    "s_addc_u32 s31, s31, 0\n\t"
    "global_load_dwordx4 a[16:19], v11, s[30:31] offset:0 sc1\n\t"
    "global_load_dwordx4 a[20:23], v11, s[30:31] offset:1024 sc1\n\t"
    "global_load_dwordx4 a[24:27], v11, s[30:31] offset:2048 sc1\n\t"
    "global_load_dwordx4 a[28:31], v11, s[30:31] offset:3072 sc1\n\t"
    "s_add_u32 s30, s30, 4096\n\t"
    "s_addc_u32 s31, s31, 0\n\t"
    "global_load_dwordx4 a[32:35], v11, s[30:31] offset:0 sc1\n\t"
    "global_load_dwordx4 a[36:39], v11, s[30:31] offset:1024 sc1\n\t"
    "v_and_b32 v87, 0x5555, v72\n\t"
    "v_and_b32 v88, 0xaaaa, v72\n\t"
    "v_bcnt_u32_b32 v87, v87, 0\n\t"
    "v_bcnt_u32_b32 v88, v88, 0\n\t"
    "v_lshl_or_b32 v78, v88, 16, v87\n\t"
    "v_mov_b32 v79, v78\n\t"
    "s_nop 1\n\t"
    "v_add_u32_dpp v79, v79, v79 row_shr:1 row_mask:0xf bank_mask:0xf bound_ctrl:0\n\t"
    "s_nop 1\n\t"
    "v_add_u32_dpp v79, v79, v79 row_shr:2 row_mask:0xf bank_mask:0xf bound_ctrl:0\n\t"
    "s_nop 1\n\t"
    "v_add_u32_dpp v79, v79, v79 row_shr:4 row_mask:0xf bank_mask:0xf bound_ctrl:0\n\t"
    "s_nop 1\n\t"
    "v_add_u32_dpp v79, v79, v79 row_shr:8 row_mask:0xf bank_mask:0xf bound_ctrl:0\n\t"
    "s_nop 1\n\t"
    "v_mov_b32_dpp v80, v79 row_newbcast:15 row_mask:0xf bank_mask:0xf\n\t"
    "v_sub_u32 v79, v79, v78\n\t"
    "v_and_b32 v81, 0xffff, v79\n\t"
    "v_lshrrev_b32 v82, 16, v79\n\t"
    "v_and_b32 v87, 0xffff, v80\n\t"
    "v_lshrrev_b32 v88, 16, v80\n\t"
    "v_sub_u32 v83, 32, v88\n\t"
    "v_max_i32 v83, 16, v83\n\t"
    "v_sub_u32 v84, 32, v87\n\t"
    "v_max_i32 v84, 16, v84\n\t"
    "v_sub_u32 v85, v87, v83\n\t"
    "v_max_i32 v85, 0, v85\n\t"
    "v_add_u32 v89, v87, v88\n\t"
    "v_subrev_u32 v89, 32, v89\n\t"
    "v_max_i32 v89, 0, v89\n\t"
    "s_nop 0\n\t"
    "v_readlane_b32 s44, v89, 0\n\t"
    "v_readlane_b32 s45, v89, 16\n\t"
    "v_readlane_b32 s46, v89, 32\n\t"
    "v_readlane_b32 s47, v89, 48\n\t"
    "v_cmp_eq_u32 vcc, 1, v71\n\t"
    "v_mov_b32 v90, s44\n\t"
    "s_nop 0\n\t"
    "v_cndmask_b32 v86, 0, v90, vcc\n\t"
    "v_cmp_eq_u32 vcc, 3, v71\n\t"
    "v_mov_b32 v90, s46\n\t"
    "s_nop 0\n\t"
    "v_cndmask_b32 v86, v86, v90, vcc\n\t"
    "v_add_u32 v86, 32, v86\n\t"
    "s_add_u32 s44, s44, s45\n\t"
    "s_add_u32 s46, s46, s47\n\t"
    "s_max_u32 s44, s44, s46\n\t"
    "s_cmp_gt_u32 s44, 0\n\t"
    "s_cselect_b32 s19, 1, 0\n\t"
    "s_cmp_gt_u32 s44, 16\n\t"
    "s_cselect_b32 s19, 2, s19\n\t"
    "s_cmp_eq_u32 s19, 2\n\t"
    "s_cbranch_scc1 .Ltabdone_%=\n\t"
    ".Ltabloop_%=:\n\t"
    "v_cmp_ne_u32 vcc, 0, v72\n\t"
    "s_and_b64 exec, exec, vcc\n\t"
    "s_cbranch_scc0 .Ltabdone_%=\n\t"
    "v_ffbl_b32 v87, v72\n\t"
    "v_add_u32 v88, -1, v72\n\t"
    "v_and_b32 v72, v72, v88\n\t"
    "v_and_b32 v88, 1, v87\n\t"
    "v_cmp_eq_u32 vcc, 1, v88\n\t"
    "v_add_u32 v93, v95, v87\n\t"
    "v_lshlrev_b32 v94, 19, v93\n\t"
    "v_add_u32 v93, v93, v96\n\t"
    "v_cndmask_b32 v89, v81, v82, vcc\n\t"
    "v_cndmask_b32 v90, v83, v84, vcc\n\t"
    "v_cndmask_b32 v91, 0, v85, vcc\n\t"
    "v_add_u32 v82, v82, v88\n\t"
    "v_xor_b32 v92, 1, v88\n\t"
    "v_add_u32 v81, v81, v92\n\t"
    "v_or_b32 v93, v93, v94\n\t"
    "v_or_b32 v94, 0x100, v94\n\t"
    "v_add3_u32 v91, v91, v86, v89\n\t"
    "v_sub_u32 v91, v91, v90\n\t"
    "v_cmp_lt_u32 s[44:45], v89, v90\n\t"
    "v_cmp_lt_u32 vcc, 15, v89\n\t"
    "v_sub_u32 v87, 31, v89\n\t"
    "s_nop 0\n\t"
    "v_cndmask_b32 v87, v89, v87, vcc\n\t"
    "v_cndmask_b32 v88, v88, v92, vcc\n\t"
    "v_lshrrev_b32 v89, 1, v87\n\t"
    "v_and_b32 v87, 1, v87\n\t"
    "v_lshl_add_u32 v87, v89, 2, v87\n\t"
    "v_lshl_add_u32 v87, v88, 1, v87\n\t"
    "v_cndmask_b32 v87, v91, v87, s[44:45]\n\t"
    "v_lshl_add_u32 v87, v87, 2, v75\n\t"
    "ds_write_b32 v87, v93\n\t"
    "v_and_b32 v88, 1, v71\n\t"
    "v_mul_i32_i24 v88, -384, v88\n\t"
    "v_add_u32 v88, 192, v88\n\t"
    "v_add_u32 v88, v88, v87\n\t"
    "v_cndmask_b32 v88, v88, v87, s[44:45]\n\t"
    "v_cndmask_b32 v94, v94, v93, s[44:45]\n\t"
    "ds_write_b32 v88, v94\n\t"
    "s_branch .Ltabloop_%=\n\t"
    ".Ltabdone_%=:\n\t"
    "s_mov_b64 exec, -1\n\t"
    "global_load_dwordx4 a[40:43], v11, s[30:31] offset:2048 sc1\n\t"
    "global_load_dwordx4 a[44:47], v11, s[30:31] offset:3072 sc1\n\t"
    "s_add_u32 s30, s30, 4096\n\t"
    "s_addc_u32 s31, s31, 0\n\t"
    "global_load_dwordx4 a[48:51], v11, s[30:31] offset:0 sc1\n\t"
    "global_load_dwordx4 a[52:55], v11, s[30:31] offset:1024 sc1\n\t"
    "global_load_dwordx4 a[56:59], v11, s[30:31] offset:2048 sc1\n\t"
    "global_load_dwordx4 a[60:63], v11, s[30:31] offset:3072 sc1\n\t"
    "s_add_u32 s30, s30, 4096\n\t"
    "s_addc_u32 s31, s31, 0\n\t"
    "s_add_u32 m0, s17, 0\n\t"
    "s_add_u32 s40, s53, 0\n\t"
    "buffer_load_dwordx4 v6, s[8:11], s40 offen offset:1024  lds\n\t"
    "s_add_u32 m0, s17, 10240\n\t"
    "s_add_u32 s40, s53, 0\n\t"
    "buffer_load_dwordx4 v7, s[8:11], s40 offen offset:1024  lds\n\t"
    "s_waitcnt lgkmcnt(0)\n\t"
    "v_lshrrev_b32 v87, 2, v69\n\t"
    "v_lshl_add_u32 v87, v87, 2, s34\n\t"
    "v_lshl_add_u32 v88, v71, 3, s34\n\t"
    "ds_read_b32 v148, v87 offset:0\n\t"
    "ds_read_b32 v152, v87 offset:192\n\t"
    "ds_read_b32 v156, v87 offset:384\n\t"
    "ds_read_b32 v160, v87 offset:576\n\t"
    "ds_read_b32 v164, v87 offset:64\n\t"
    "ds_read_b32 v168, v87 offset:256\n\t"
    "ds_read_b32 v172, v87 offset:448\n\t"
    "ds_read_b32 v176, v87 offset:640\n\t"
    "ds_read_b32 v180, v87 offset:128\n\t"
    "ds_read_b32 v184, v87 offset:320\n\t"
    "ds_read_b32 v188, v87 offset:512\n\t"
    "ds_read_b32 v192, v87 offset:704\n\t"
    "ds_read2_b64 v[196:199], v88 offset0:0 offset1:4\n\t"
    "ds_read2_b64 v[200:203], v88 offset0:8 offset1:12\n\t"
    "ds_read2_b64 v[204:207], v88 offset0:24 offset1:28\n\t"
    "ds_read2_b64 v[208:211], v88 offset0:32 offset1:36\n\t"
    "ds_read2_b64 v[212:215], v88 offset0:48 offset1:52\n\t"
    "ds_read2_b64 v[216:219], v88 offset0:56 offset1:60\n\t"
    "ds_read2_b64 v[220:223], v88 offset0:72 offset1:76\n\t"
    "ds_read2_b64 v[224:227], v88 offset0:80 offset1:84\n\t"
    "ds_read2_b64 v[228:231], v88 offset0:16 offset1:20\n\t"
    "ds_read2_b64 v[232:235], v88 offset0:64 offset1:68\n\t"
    "s_add_u32 m0, s17, 20480\n\t"
    "s_add_u32 s40, s53, 0\n\t"
    "buffer_load_dwordx4 v8, s[8:11], s40 offen offset:1024  lds\n\t"
    "s_add_u32 m0, s17, 30720\n\t"
    "s_add_u32 s40, s53, 0\n\t"
    "buffer_load_dwordx4 v9, s[8:11], s40 offen offset:1024  lds\n\t"
    "s_add_u32 m0, s17, 2048\n\t"
    "s_add_u32 s40, s53, 32768\n\t"
    "buffer_load_dwordx4 v6, s[8:11], s40 offen offset:1024  lds\n\t"
    "s_waitcnt lgkmcnt(0)\n\t"
    "v_mad_u32_u16 v148, v148, 64, v97\n\t"
    "v_mad_u32_u16 v152, v152, 64, v97\n\t"
    "v_mad_u32_u16 v156, v156, 64, v97\n\t"
    "v_mad_u32_u16 v160, v160, 64, v97\n\t"
    "v_mad_u32_u16 v164, v164, 64, v97\n\t"
    "v_mad_u32_u16 v168, v168, 64, v97\n\t"
    "s_add_u32 m0, s17, 12288\n\t"
    "s_add_u32 s40, s53, 32768\n\t"
    "buffer_load_dwordx4 v7, s[8:11], s40 offen offset:1024  lds\n\t"
    "v_mad_u32_u16 v172, v172, 64, v97\n\t"
    "v_mad_u32_u16 v176, v176, 64, v97\n\t"
    "v_mad_u32_u16 v180, v180, 64, v97\n\t"
    "v_mad_u32_u16 v184, v184, 64, v97\n\t"
    "v_mad_u32_u16 v188, v188, 64, v97\n\t"
    "v_mad_u32_u16 v192, v192, 64, v97\n\t"
    "s_add_u32 m0, s17, 22528\n\t"
    "s_add_u32 s40, s53, 32768\n\t"
    "buffer_load_dwordx4 v8, s[8:11], s40 offen offset:1024  lds\n\t"
    "s_add_u32 m0, s17, 32768\n\t"
    "s_add_u32 s40, s53, 32768\n\t"
    "buffer_load_dwordx4 v9, s[8:11], s40 offen offset:1024  lds\n\t"
    "s_add_u32 m0, s17, 4096\n\t"
    "s_add_u32 s40, s53, 65536\n\t"
    "buffer_load_dwordx4 v6, s[8:11], s40 offen offset:1024  lds\n\t"
    "s_add_u32 m0, s17, 14336\n\t"
    "s_add_u32 s40, s53, 65536\n\t"
    "buffer_load_dwordx4 v7, s[8:11], s40 offen offset:1024  lds\n\t"
    "s_add_u32 m0, s17, 24576\n\t"
    "s_add_u32 s40, s53, 65536\n\t"
    "buffer_load_dwordx4 v8, s[8:11], s40 offen offset:1024  lds\n\t"
    "s_add_u32 m0, s17, 34816\n\t"
    "s_add_u32 s40, s53, 65536\n\t"
    "buffer_load_dwordx4 v9, s[8:11], s40 offen offset:1024  lds\n\t"
    "s_barrier\n\t"
    "s_cmp_eq_u32 s19, 1\n\t"
    "s_cbranch_scc1 .LvarB_%=\n\t"
    "s_cmp_eq_u32 s19, 2\n\t"
    "s_cbranch_scc1 .LvarC_%=\n\t"
    "ds_read_b128 v[76:79], v2 offset:0\n\t"
    "ds_read_b128 v[80:83], v3 offset:0\n\t"
    "ds_read_b128 v[84:87], v2 offset:2048\n\t"
    "s_waitcnt lgkmcnt(1)\n\t"
    "s_waitcnt vmcnt(27)\n\t"
    "v_lshrrev_b32 v196, 16, v196\n\t"
    "v_smfmac_f32_16x16x64_bf16 v[100:103], a[0:3], v[76:83], v12\n\t"
    "ds_read_b128 v[88:91], v3 offset:2048\n\t"
    "ds_read_b128 v[92:95], v2 offset:4096\n\t"
    "s_waitcnt vmcnt(26)\n\t"
    "v_xad_u32 v196, v196, v253, v252\n\t"
    "v_smfmac_f32_16x16x64_bf16 v[104:107], a[4:7], v[76:83], v12 abid:1\n\t"
    "ds_read_b128 v[96:99], v3 offset:4096\n\t"
    "ds_read_b128 v[60:63], v2 offset:6144\n\t"
    "s_add_u32 m0, s17, 6144\n\t"
    "s_add_u32 s40, s53, 98304\n\t"
    "buffer_load_dwordx4 v6, s[8:11], s40 offen offset:1024  lds\n\t"
    "s_waitcnt vmcnt(26)\n\t"
    "v_lshrrev_b32 v197, 16, v197\n\t"
    "v_smfmac_f32_16x16x64_bf16 v[108:111], a[8:11], v[76:83], v13\n\t"
    "ds_read_b128 v[64:67], v3 offset:6144\n\t"
    "s_waitcnt vmcnt(25)\n\t"
    "v_xad_u32 v197, v197, v253, v252\n\t"
    "v_smfmac_f32_16x16x64_bf16 v[112:115], a[12:15], v[76:83], v13 abid:1\n\t"
    "s_add_u32 m0, s17, 16384\n\t"
    "s_add_u32 s40, s53, 98304\n\t"
    "buffer_load_dwordx4 v7, s[8:11], s40 offen offset:1024  lds\n\t"
    "s_waitcnt lgkmcnt(4)\n\t"
    "v_lshrrev_b32 v198, 16, v198\n\t"
    "v_smfmac_f32_16x16x64_bf16 v[116:119], a[0:3], v[84:91], v12\n\t"
    "ds_read_b128 v[68:71], v4 offset:0\n\t"
    "v_xad_u32 v198, v198, v253, v252\n\t"
    "v_smfmac_f32_16x16x64_bf16 v[120:123], a[4:7], v[84:91], v12 abid:1\n\t"
    "ds_read_b128 v[72:75], v5 offset:0\n\t"
    "s_add_u32 m0, s17, 26624\n\t"
    "s_add_u32 s40, s53, 98304\n\t"
    "buffer_load_dwordx4 v8, s[8:11], s40 offen offset:1024  lds\n\t"
    "v_lshrrev_b32 v199, 16, v199\n\t"
    "v_smfmac_f32_16x16x64_bf16 v[124:127], a[8:11], v[84:91], v13\n\t"
    "ds_read_b128 v[76:79], v4 offset:2048\n\t"
    "v_xad_u32 v199, v199, v253, v252\n\t"
    "v_smfmac_f32_16x16x64_bf16 v[128:131], a[12:15], v[84:91], v13 abid:1\n\t"
    "ds_read_b128 v[80:83], v5 offset:2048\n\t"
    "s_waitcnt lgkmcnt(6)\n\t"
    "v_lshrrev_b32 v200, 16, v200\n\t"
    "v_smfmac_f32_16x16x64_bf16 v[236:239], a[0:3], v[92:99], v12\n\t"
    "s_add_u32 m0, s17, 36864\n\t"
    "s_add_u32 s40, s53, 98304\n\t"
    "buffer_load_dwordx4 v9, s[8:11], s40 offen offset:1024  lds\n\t"
    "ds_read_b128 v[84:87], v4 offset:4096\n\t"
    "v_xad_u32 v200, v200, v253, v252\n\t"
    "v_smfmac_f32_16x16x64_bf16 v[240:243], a[4:7], v[92:99], v12 abid:1\n\t"
    "ds_read_b128 v[88:91], v5 offset:4096\n\t"
    "v_lshrrev_b32 v201, 16, v201\n\t"
    "v_smfmac_f32_16x16x64_bf16 v[244:247], a[8:11], v[92:99], v13\n\t"
    "v_xad_u32 v201, v201, v253, v252\n\t"
    "v_smfmac_f32_16x16x64_bf16 v[248:251], a[12:15], v[92:99], v13 abid:1\n\t"
    "global_load_dwordx4 a[64:67], v11, s[30:31] offset:0 sc1\n\t"
    "s_waitcnt lgkmcnt(5)\n\t"
    "v_lshrrev_b32 v202, 16, v202\n\t"
    "v_smfmac_f32_16x16x64_bf16 v[44:47], a[0:3], v[60:67], v12\n\t"
    "ds_read_b128 v[92:95], v4 offset:6144\n\t"
    "v_xad_u32 v202, v202, v253, v252\n\t"
    "v_smfmac_f32_16x16x64_bf16 v[48:51], a[4:7], v[60:67], v12 abid:1\n\t"
    "ds_read_b128 v[96:99], v5 offset:6144\n\t"
    "v_lshrrev_b32 v203, 16, v203\n\t"
    "v_smfmac_f32_16x16x64_bf16 v[52:55], a[8:11], v[60:67], v13\n\t"
    "global_load_dwordx4 a[68:71], v11, s[30:31] offset:1024 sc1\n\t"
    "v_xad_u32 v203, v203, v253, v252\n\t"
    "v_smfmac_f32_16x16x64_bf16 v[56:59], a[12:15], v[60:67], v13 abid:1\n\t"
    "s_waitcnt lgkmcnt(3)\n\t"
    "s_waitcnt vmcnt(29)\n\t"
    "v_lshrrev_b32 v204, 16, v204\n\t"
    "v_smfmac_f32_16x16x64_bf16 v[100:103], a[16:19], v[68:75], v14\n\t"
    "ds_read_b128 v[60:63], v2 offset:256\n\t"
    "s_waitcnt vmcnt(28)\n\t"
    "v_xad_u32 v204, v204, v253, v252\n\t"
    "v_smfmac_f32_16x16x64_bf16 v[104:107], a[20:23], v[68:75], v14 abid:1\n\t"
    "global_load_dwordx4 a[72:75], v11, s[30:31] offset:2048 sc1\n\t"
    "s_waitcnt vmcnt(28)\n\t"
    "v_lshrrev_b32 v205, 16, v205\n\t"
    "v_smfmac_f32_16x16x64_bf16 v[108:111], a[24:27], v[68:75], v15\n\t"
    "ds_read_b128 v[64:67], v3 offset:256\n\t"
    "s_waitcnt vmcnt(27)\n\t"
    "v_xad_u32 v205, v205, v253, v252\n\t"
    "v_smfmac_f32_16x16x64_bf16 v[112:115], a[28:31], v[68:75], v15 abid:1\n\t"
    "ds_read_b128 v[68:71], v2 offset:2304\n\t"
    "v_lshrrev_b32 v206, 16, v206\n\t"
    "v_smfmac_f32_16x16x64_bf16 v[116:119], a[16:19], v[76:83], v14\n\t"
    "global_load_dwordx4 a[76:79], v11, s[30:31] offset:3072 sc1\n\t"
    "s_add_u32 s30, s30, 4096\n\t"
    "s_addc_u32 s31, s31, 0\n\t"
    "ds_read_b128 v[72:75], v3 offset:2304\n\t"
    "v_xad_u32 v206, v206, v253, v252\n\t"
    "v_smfmac_f32_16x16x64_bf16 v[120:123], a[20:23], v[76:83], v14 abid:1\n\t"
    "v_lshrrev_b32 v207, 16, v207\n\t"
    "v_smfmac_f32_16x16x64_bf16 v[124:127], a[24:27], v[76:83], v15\n\t"
    "v_xad_u32 v207, v207, v253, v252\n\t"
    "v_smfmac_f32_16x16x64_bf16 v[128:131], a[28:31], v[76:83], v15 abid:1\n\t"
    "global_load_dwordx4 a[80:83], v11, s[30:31] offset:0 sc1\n\t"
    "s_waitcnt lgkmcnt(3)\n\t"
    "v_lshrrev_b32 v208, 16, v208\n\t"
    "v_smfmac_f32_16x16x64_bf16 v[236:239], a[16:19], v[84:91], v14\n\t"
    "ds_read_b128 v[76:79], v2 offset:4352\n\t"
    "v_xad_u32 v208, v208, v253, v252\n\t"
    "v_smfmac_f32_16x16x64_bf16 v[240:243], a[20:23], v[84:91], v14 abid:1\n\t"
    "ds_read_b128 v[80:83], v3 offset:4352\n\t"
    "v_lshrrev_b32 v209, 16, v209\n\t"
    "v_smfmac_f32_16x16x64_bf16 v[244:247], a[24:27], v[84:91], v15\n\t"
    "global_load_dwordx4 a[84:87], v11, s[30:31] offset:1024 sc1\n\t"
    "v_xad_u32 v209, v209, v253, v252\n\t"
    "v_smfmac_f32_16x16x64_bf16 v[248:251], a[28:31], v[84:91], v15 abid:1\n\t"
    "ds_read_b128 v[84:87], v2 offset:6400\n\t"
    "v_lshrrev_b32 v210, 16, v210\n\t"
    "v_smfmac_f32_16x16x64_bf16 v[44:47], a[16:19], v[92:99], v14\n\t"
    "ds_read_b128 v[88:91], v3 offset:6400\n\t"
    "v_xad_u32 v210, v210, v253, v252\n\t"
    "v_smfmac_f32_16x16x64_bf16 v[48:51], a[20:23], v[92:99], v14 abid:1\n\t"
    "global_load_dwordx4 a[88:91], v11, s[30:31] offset:2048 sc1\n\t"
    "v_lshrrev_b32 v211, 16, v211\n\t"
    "v_smfmac_f32_16x16x64_bf16 v[52:55], a[24:27], v[92:99], v15\n\t"
    "v_xad_u32 v211, v211, v253, v252\n\t"
    "v_smfmac_f32_16x16x64_bf16 v[56:59], a[28:31], v[92:99], v15 abid:1\n\t"
    "s_waitcnt lgkmcnt(3)\n\t"
    "s_waitcnt vmcnt(30)\n\t"
    "v_lshrrev_b32 v212, 16, v212\n\t"
    "v_smfmac_f32_16x16x64_bf16 v[100:103], a[32:35], v[60:67], v16\n\t"
    "global_load_dwordx4 a[92:95], v11, s[30:31] offset:3072 sc1\n\t"
    "s_add_u32 s30, s30, 4096\n\t"
    "s_addc_u32 s31, s31, 0\n\t"
    "s_waitcnt vmcnt(30)\n\t"
    "v_xad_u32 v212, v212, v253, v252\n\t"
    "v_smfmac_f32_16x16x64_bf16 v[104:107], a[36:39], v[60:67], v16 abid:1\n\t"
    "ds_read_b128 v[92:95], v4 offset:256\n\t"
    "s_waitcnt vmcnt(29)\n\t"
    "v_lshrrev_b32 v213, 16, v213\n\t"
    "v_smfmac_f32_16x16x64_bf16 v[108:111], a[40:43], v[60:67], v17\n\t"
    "ds_read_b128 v[96:99], v5 offset:256\n\t"
    "s_waitcnt vmcnt(28)\n\t"
    "v_xad_u32 v213, v213, v253, v252\n\t"
    "v_smfmac_f32_16x16x64_bf16 v[112:115], a[44:47], v[60:67], v17 abid:1\n\t"
    "global_load_dwordx4 a[96:99], v11, s[30:31] offset:0 sc1\n\t"
    "ds_read_b128 v[60:63], v4 offset:2304\n\t"
    "v_lshrrev_b32 v214, 16, v214\n\t"
    "v_smfmac_f32_16x16x64_bf16 v[116:119], a[32:35], v[68:75], v16\n\t"
    "ds_read_b128 v[64:67], v5 offset:2304\n\t"
    "v_xad_u32 v214, v214, v253, v252\n\t"
    "v_smfmac_f32_16x16x64_bf16 v[120:123], a[36:39], v[68:75], v16 abid:1\n\t"
    "v_lshrrev_b32 v215, 16, v215\n\t"
    "v_smfmac_f32_16x16x64_bf16 v[124:127], a[40:43], v[68:75], v17\n\t"
    "global_load_dwordx4 a[100:103], v11, s[30:31] offset:1024 sc1\n\t"
    "v_xad_u32 v215, v215, v253, v252\n\t"
    "v_smfmac_f32_16x16x64_bf16 v[128:131], a[44:47], v[68:75], v17 abid:1\n\t"
    "ds_read_b128 v[68:71], v4 offset:4352\n\t"
    "s_waitcnt lgkmcnt(5)\n\t"
    "v_lshrrev_b32 v216, 16, v216\n\t"
    "v_smfmac_f32_16x16x64_bf16 v[236:239], a[32:35], v[76:83], v16\n\t"
    "ds_read_b128 v[72:75], v5 offset:4352\n\t"
    "v_xad_u32 v216, v216, v253, v252\n\t"
    "v_smfmac_f32_16x16x64_bf16 v[240:243], a[36:39], v[76:83], v16 abid:1\n\t"
    "global_load_dwordx4 a[104:107], v11, s[30:31] offset:2048 sc1\n\t"
    "v_lshrrev_b32 v217, 16, v217\n\t"
    "v_smfmac_f32_16x16x64_bf16 v[244:247], a[40:43], v[76:83], v17\n\t"
    "v_xad_u32 v217, v217, v253, v252\n\t"
    "v_smfmac_f32_16x16x64_bf16 v[248:251], a[44:47], v[76:83], v17 abid:1\n\t"
    "ds_read_b128 v[76:79], v4 offset:6400\n\t"
    "v_lshrrev_b32 v218, 16, v218\n\t"
    "v_smfmac_f32_16x16x64_bf16 v[44:47], a[32:35], v[84:91], v16\n\t"
    "global_load_dwordx4 a[108:111], v11, s[30:31] offset:3072 sc1\n\t"
    "s_add_u32 s30, s30, 4096\n\t"
    "s_addc_u32 s31, s31, 0\n\t"
    "ds_read_b128 v[80:83], v5 offset:6400\n\t"
    "v_xad_u32 v218, v218, v253, v252\n\t"
    "v_smfmac_f32_16x16x64_bf16 v[48:51], a[36:39], v[84:91], v16 abid:1\n\t"
    "v_lshrrev_b32 v219, 16, v219\n\t"
    "v_smfmac_f32_16x16x64_bf16 v[52:55], a[40:43], v[84:91], v17\n\t"
    "v_xad_u32 v219, v219, v253, v252\n\t"
    "v_smfmac_f32_16x16x64_bf16 v[56:59], a[44:47], v[84:91], v17 abid:1\n\t"
    "s_waitcnt lgkmcnt(2)\n\t"
    "s_waitcnt vmcnt(31)\n\t"
    "v_lshrrev_b32 v220, 16, v220\n\t"
    "v_smfmac_f32_16x16x64_bf16 v[100:103], a[48:51], v[92:99], v18\n\t"
    "global_load_dwordx4 a[112:115], v11, s[30:31] offset:0 sc1\n\t"
    "s_waitcnt vmcnt(31)\n\t"
    "v_xad_u32 v220, v220, v253, v252\n\t"
    "v_smfmac_f32_16x16x64_bf16 v[104:107], a[52:55], v[92:99], v18 abid:1\n\t"
    "ds_read_b128 v[84:87], v2 offset:512\n\t"
    "s_waitcnt vmcnt(30)\n\t"
    "v_lshrrev_b32 v221, 16, v221\n\t"
    "v_smfmac_f32_16x16x64_bf16 v[108:111], a[56:59], v[92:99], v19\n\t"
    "global_load_dwordx4 a[116:119], v11, s[30:31] offset:1024 sc1\n\t"
    "s_waitcnt vmcnt(30)\n\t"
    "v_xad_u32 v221, v221, v253, v252\n\t"
    "v_smfmac_f32_16x16x64_bf16 v[112:115], a[60:63], v[92:99], v19 abid:1\n\t"
    "ds_read_b128 v[88:91], v3 offset:512\n\t"
    "v_lshrrev_b32 v222, 16, v222\n\t"
    "v_smfmac_f32_16x16x64_bf16 v[116:119], a[48:51], v[60:67], v18\n\t"
    "ds_read_b128 v[92:95], v2 offset:2560\n\t"
    "v_xad_u32 v222, v222, v253, v252\n\t"
    "v_smfmac_f32_16x16x64_bf16 v[120:123], a[52:55], v[60:67], v18 abid:1\n\t"
    "global_load_dwordx4 a[120:123], v11, s[30:31] offset:2048 sc1\n\t"
    "ds_read_b128 v[96:99], v3 offset:2560\n\t"
    "v_lshrrev_b32 v223, 16, v223\n\t"
    "v_smfmac_f32_16x16x64_bf16 v[124:127], a[56:59], v[60:67], v19\n\t"
    "v_xad_u32 v223, v223, v253, v252\n\t"
    "v_smfmac_f32_16x16x64_bf16 v[128:131], a[60:63], v[60:67], v19 abid:1\n\t"
    "ds_read_b128 v[60:63], v2 offset:4608\n\t"
    "v_lshrrev_b32 v224, 16, v224\n\t"
    "v_smfmac_f32_16x16x64_bf16 v[236:239], a[48:51], v[68:75], v18\n\t"
    "global_load_dwordx4 a[124:127], v11, s[30:31] offset:3072 sc1\n\t"
    "s_add_u32 s30, s30, 4096\n\t"
    "s_addc_u32 s31, s31, 0\n\t"
    "ds_read_b128 v[64:67], v3 offset:4608\n\t"
    "v_xad_u32 v224, v224, v253, v252\n\t"
    "v_smfmac_f32_16x16x64_bf16 v[240:243], a[52:55], v[68:75], v18 abid:1\n\t"
    "v_lshrrev_b32 v225, 16, v225\n\t"
    "v_smfmac_f32_16x16x64_bf16 v[244:247], a[56:59], v[68:75], v19\n\t"
    "v_xad_u32 v225, v225, v253, v252\n\t"
    "v_smfmac_f32_16x16x64_bf16 v[248:251], a[60:63], v[68:75], v19 abid:1\n\t"
    "global_load_dwordx4 v[148:151], v148, s[56:57] sc1\n\t"
    "s_waitcnt lgkmcnt(3)\n\t"
    "v_lshrrev_b32 v226, 16, v226\n\t"
    "v_smfmac_f32_16x16x64_bf16 v[44:47], a[48:51], v[76:83], v18\n\t"
    "ds_read_b128 v[68:71], v2 offset:6656\n\t"
    "v_xad_u32 v226, v226, v253, v252\n\t"
    "v_smfmac_f32_16x16x64_bf16 v[48:51], a[52:55], v[76:83], v18 abid:1\n\t"
    "ds_read_b128 v[72:75], v3 offset:6656\n\t"
    "v_lshrrev_b32 v227, 16, v227\n\t"
    "v_smfmac_f32_16x16x64_bf16 v[52:55], a[56:59], v[76:83], v19\n\t"
    "global_load_dwordx4 v[152:155], v152, s[56:57] sc1\n\t"
    "v_xad_u32 v227, v227, v253, v252\n\t"
    "v_smfmac_f32_16x16x64_bf16 v[56:59], a[60:63], v[76:83], v19 abid:1\n\t"
    "ds_read_b128 v[76:79], v4 offset:512\n\t"
    "s_waitcnt vmcnt(17)\n\t"
    "v_smfmac_f32_16x16x64_bf16 v[100:103], a[64:67], v[84:91], v20\n\t"
    "ds_read_b128 v[80:83], v5 offset:512\n\t"
    "s_waitcnt vmcnt(16)\n\t"
    "v_smfmac_f32_16x16x64_bf16 v[104:107], a[68:71], v[84:91], v20 abid:1\n\t"
    "global_load_dwordx4 v[156:159], v156, s[56:57] sc1\n\t"
    "s_waitcnt vmcnt(16)\n\t"
    "v_smfmac_f32_16x16x64_bf16 v[108:111], a[72:75], v[84:91], v21\n\t"
    "s_waitcnt vmcnt(15)\n\t"
    "v_smfmac_f32_16x16x64_bf16 v[112:115], a[76:79], v[84:91], v21 abid:1\n\t"
    "ds_read_b128 v[84:87], v4 offset:2560\n\t"
    "s_waitcnt lgkmcnt(4)\n\t"
    "v_smfmac_f32_16x16x64_bf16 v[116:119], a[64:67], v[92:99], v20\n\t"
    "global_load_dwordx4 v[160:163], v160, s[56:57] sc1\n\t"
    "ds_read_b128 v[88:91], v5 offset:2560\n\t"
    "v_smfmac_f32_16x16x64_bf16 v[120:123], a[68:71], v[92:99], v20 abid:1\n\t"
    "v_smfmac_f32_16x16x64_bf16 v[124:127], a[72:75], v[92:99], v21\n\t"
    "v_smfmac_f32_16x16x64_bf16 v[128:131], a[76:79], v[92:99], v21 abid:1\n\t"
    "global_load_dwordx4 v[164:167], v164, s[56:57] sc1\n\t"
    "ds_read_b128 v[92:95], v4 offset:4608\n\t"
    "v_smfmac_f32_16x16x64_bf16 v[236:239], a[64:67], v[60:67], v20\n\t"
    "ds_read_b128 v[96:99], v5 offset:4608\n\t"
    "v_smfmac_f32_16x16x64_bf16 v[240:243], a[68:71], v[60:67], v20 abid:1\n\t"
    "v_smfmac_f32_16x16x64_bf16 v[244:247], a[72:75], v[60:67], v21\n\t"
    "global_load_dwordx4 v[168:171], v168, s[56:57] sc1\n\t"
    "v_smfmac_f32_16x16x64_bf16 v[248:251], a[76:79], v[60:67], v21 abid:1\n\t"
    "ds_read_b128 v[60:63], v4 offset:6656\n\t"
    "s_waitcnt lgkmcnt(3)\n\t"
    "v_smfmac_f32_16x16x64_bf16 v[44:47], a[64:67], v[68:75], v20\n\t"
    "ds_read_b128 v[64:67], v5 offset:6656\n\t"
    "v_smfmac_f32_16x16x64_bf16 v[48:51], a[68:71], v[68:75], v20 abid:1\n\t"
    "global_load_dwordx4 v[172:175], v172, s[56:57] sc1\n\t"
    "v_smfmac_f32_16x16x64_bf16 v[52:55], a[72:75], v[68:75], v21\n\t"
    "v_smfmac_f32_16x16x64_bf16 v[56:59], a[76:79], v[68:75], v21 abid:1\n\t"
    "ds_read_b128 v[68:71], v2 offset:768\n\t"
    "s_waitcnt vmcnt(18)\n\t"
    "v_smfmac_f32_16x16x64_bf16 v[100:103], a[80:83], v[76:83], v22\n\t"
    "global_load_dwordx4 v[176:179], v176, s[56:57] sc1\n\t"
    "ds_read_b128 v[72:75], v3 offset:768\n\t"
    "s_waitcnt vmcnt(18)\n\t"
    "v_smfmac_f32_16x16x64_bf16 v[104:107], a[84:87], v[76:83], v22 abid:1\n\t"
    "s_waitcnt vmcnt(17)\n\t"
    "v_smfmac_f32_16x16x64_bf16 v[108:111], a[88:91], v[76:83], v23\n\t"
    "s_waitcnt vmcnt(16)\n\t"
    "v_smfmac_f32_16x16x64_bf16 v[112:115], a[92:95], v[76:83], v23 abid:1\n\t"
    "global_load_dwordx4 v[28:31], v11, s[36:37] offset:0 sc1\n\t"
    "ds_read_b128 v[76:79], v2 offset:2816\n\t"
    "v_smfmac_f32_16x16x64_bf16 v[116:119], a[80:83], v[84:91], v22\n\t"
    "ds_read_b128 v[80:83], v3 offset:2816\n\t"
    "v_smfmac_f32_16x16x64_bf16 v[120:123], a[84:87], v[84:91], v22 abid:1\n\t"
    "v_smfmac_f32_16x16x64_bf16 v[124:127], a[88:91], v[84:91], v23\n\t"
    "global_load_dwordx4 v[32:35], v11, s[36:37] offset:1024 sc1\n\t"
    "v_smfmac_f32_16x16x64_bf16 v[128:131], a[92:95], v[84:91], v23 abid:1\n\t"
    "ds_read_b128 v[84:87], v2 offset:4864\n\t"
    "s_waitcnt lgkmcnt(3)\n\t"
    "v_smfmac_f32_16x16x64_bf16 v[236:239], a[80:83], v[92:99], v22\n\t"
    "ds_read_b128 v[88:91], v3 offset:4864\n\t"
    "v_smfmac_f32_16x16x64_bf16 v[240:243], a[84:87], v[92:99], v22 abid:1\n\t"
    "global_load_dwordx4 v[36:39], v11, s[36:37] offset:2048 sc1\n\t"
    "v_smfmac_f32_16x16x64_bf16 v[244:247], a[88:91], v[92:99], v23\n\t"
    "v_smfmac_f32_16x16x64_bf16 v[248:251], a[92:95], v[92:99], v23 abid:1\n\t"
    "ds_read_b128 v[92:95], v2 offset:6912\n\t"
    "v_smfmac_f32_16x16x64_bf16 v[44:47], a[80:83], v[60:67], v22\n\t"
    "global_load_dwordx4 v[40:43], v11, s[36:37] offset:3072 sc1\n\t"
    "ds_read_b128 v[96:99], v3 offset:6912\n\t"
    "v_smfmac_f32_16x16x64_bf16 v[48:51], a[84:87], v[60:67], v22 abid:1\n\t"
    "v_smfmac_f32_16x16x64_bf16 v[52:55], a[88:91], v[60:67], v23\n\t"
    "v_smfmac_f32_16x16x64_bf16 v[56:59], a[92:95], v[60:67], v23 abid:1\n\t"
    "global_load_dwordx4 a[128:131], v11, s[30:31] offset:0 sc1\n\t"
    "ds_read_b128 v[60:63], v4 offset:768\n\t"
    "s_waitcnt vmcnt(20)\n\t"
    "v_smfmac_f32_16x16x64_bf16 v[100:103], a[96:99], v[68:75], v24\n\t"
    "ds_read_b128 v[64:67], v5 offset:768\n\t"
    "s_waitcnt vmcnt(19)\n\t"
    "v_smfmac_f32_16x16x64_bf16 v[104:107], a[100:103], v[68:75], v24 abid:1\n\t"
    "s_waitcnt vmcnt(18)\n\t"
    "v_smfmac_f32_16x16x64_bf16 v[108:111], a[104:107], v[68:75], v25\n\t"
    "global_load_dwordx4 a[132:135], v11, s[30:31] offset:1024 sc1\n\t"
    "s_waitcnt vmcnt(18)\n\t"
    "v_smfmac_f32_16x16x64_bf16 v[112:115], a[108:111], v[68:75], v25 abid:1\n\t"
    "ds_read_b128 v[68:71], v4 offset:2816\n\t"
    "s_waitcnt lgkmcnt(3)\n\t"
    "v_smfmac_f32_16x16x64_bf16 v[116:119], a[96:99], v[76:83], v24\n\t"
    "ds_read_b128 v[72:75], v5 offset:2816\n\t"
    "v_smfmac_f32_16x16x64_bf16 v[120:123], a[100:103], v[76:83], v24 abid:1\n\t"
    "global_load_dwordx4 a[136:139], v11, s[30:31] offset:2048 sc1\n\t"
    "s_waitcnt vmcnt(14)\n\t"
    "ds_write_b128 v255, v[148:151] offset:0\n\t"
    "ds_read_b32 v148, v254 offset:0\n\t"
    "v_smfmac_f32_16x16x64_bf16 v[124:127], a[104:107], v[76:83], v25\n\t"
    "ds_read_b32 v149, v254 offset:64\n\t"
    "v_smfmac_f32_16x16x64_bf16 v[128:131], a[108:111], v[76:83], v25 abid:1\n\t"
    "ds_read_b128 v[76:79], v4 offset:4864\n\t"
    "v_smfmac_f32_16x16x64_bf16 v[236:239], a[96:99], v[84:91], v24\n\t"
    "global_load_dwordx4 a[140:143], v11, s[30:31] offset:3072 sc1\n\t"
    "s_add_u32 s30, s30, 4096\n\t"
    "s_addc_u32 s31, s31, 0\n\t"
    "ds_read_b128 v[80:83], v5 offset:4864\n\t"
    "v_smfmac_f32_16x16x64_bf16 v[240:243], a[100:103], v[84:91], v24 abid:1\n\t"
    "ds_read_b32 v150, v254 offset:128\n\t"
    "v_smfmac_f32_16x16x64_bf16 v[244:247], a[104:107], v[84:91], v25\n\t"
    "ds_read_b32 v151, v254 offset:192\n\t"
    "v_smfmac_f32_16x16x64_bf16 v[248:251], a[108:111], v[84:91], v25 abid:1\n\t"
    "global_load_dwordx4 a[144:147], v11, s[30:31] offset:0 sc1\n\t"
    "ds_read_b128 v[84:87], v4 offset:6912\n\t"
    "v_smfmac_f32_16x16x64_bf16 v[44:47], a[96:99], v[92:99], v24\n\t"
    "ds_read_b128 v[88:91], v5 offset:6912\n\t"
    "v_smfmac_f32_16x16x64_bf16 v[48:51], a[100:103], v[92:99], v24 abid:1\n\t"
    "s_waitcnt vmcnt(15)\n\t"
    "ds_write_b128 v255, v[152:155] offset:1024\n\t"
    "ds_read_b32 v152, v254 offset:1024\n\t"
    "v_smfmac_f32_16x16x64_bf16 v[52:55], a[104:107], v[92:99], v25\n\t"
    "global_load_dwordx4 a[148:151], v11, s[30:31] offset:1024 sc1\n\t"
    "ds_read_b32 v153, v254 offset:1088\n\t"
    "v_smfmac_f32_16x16x64_bf16 v[56:59], a[108:111], v[92:99], v25 abid:1\n\t"
    "ds_read_b32 v154, v254 offset:1152\n\t"
    "s_waitcnt lgkmcnt(8)\n\t"
    "v_smfmac_f32_16x16x64_bf16 v[100:103], a[112:115], v[60:67], v26\n\t"
    "ds_read_b32 v155, v254 offset:1216\n\t"
    "v_smfmac_f32_16x16x64_bf16 v[104:107], a[116:119], v[60:67], v26 abid:1\n\t"
    "global_load_dwordx4 a[152:155], v11, s[30:31] offset:2048 sc1\n\t"
    "s_waitcnt vmcnt(16)\n\t"
    "ds_write_b128 v255, v[156:159] offset:10240\n\t"
    "ds_read_b32 v156, v254 offset:10240\n\t"
    "v_smfmac_f32_16x16x64_bf16 v[108:111], a[120:123], v[60:67], v27\n\t"
    "ds_read_b32 v157, v254 offset:10304\n\t"
    "v_smfmac_f32_16x16x64_bf16 v[112:115], a[124:127], v[60:67], v27 abid:1\n\t"
    "s_barrier\n\t"
    "ds_read_b128 v[92:95], v2 offset:1024\n\t"
    "v_smfmac_f32_16x16x64_bf16 v[116:119], a[112:115], v[68:75], v26\n\t"
    "ds_read_b128 v[96:99], v3 offset:1024\n\t"
    "global_load_dwordx4 a[156:159], v11, s[30:31] offset:3072 sc1\n\t"
    "s_add_u32 s30, s30, 4096\n\t"
    "s_addc_u32 s31, s31, 0\n\t"
    "v_smfmac_f32_16x16x64_bf16 v[120:123], a[116:119], v[68:75], v26 abid:1\n\t"
    "ds_read_b32 v158, v254 offset:10368\n\t"
    "v_smfmac_f32_16x16x64_bf16 v[124:127], a[120:123], v[68:75], v27\n\t"
    "ds_read_b32 v159, v254 offset:10432\n\t"
    "v_smfmac_f32_16x16x64_bf16 v[128:131], a[124:127], v[68:75], v27 abid:1\n\t"
    "s_waitcnt vmcnt(16)\n\t"
    "ds_write_b128 v255, v[160:163] offset:11264\n\t"
    "ds_read_b32 v160, v254 offset:11264\n\t"
    "global_load_dwordx4 a[160:163], v11, s[30:31] offset:0 sc1\n\t"
    "v_smfmac_f32_16x16x64_bf16 v[236:239], a[112:115], v[76:83], v26\n\t"
    "ds_read_b32 v161, v254 offset:11328\n\t"
    "v_smfmac_f32_16x16x64_bf16 v[240:243], a[116:119], v[76:83], v26 abid:1\n\t"
    "ds_read_b32 v162, v254 offset:11392\n\t"
    "ds_read_b32 v163, v254 offset:11456\n\t"
    "ds_read_b64 v[60:61], v196 offset:0\n\t"
    "v_smfmac_f32_16x16x64_bf16 v[244:247], a[120:123], v[76:83], v27\n\t"
    "ds_read_b64 v[62:63], v197 offset:0\n\t"
    "ds_read_b64 v[64:65], v198 offset:0\n\t"
    "ds_read_b64 v[66:67], v199 offset:0\n\t"
    "ds_read_b128 v[68:71], v2 offset:3072\n\t"
    "v_smfmac_f32_16x16x64_bf16 v[248:251], a[124:127], v[76:83], v27 abid:1\n\t"
    "ds_read_b128 v[72:75], v3 offset:3072\n\t"
    "s_waitcnt vmcnt(16)\n\t"
    "ds_write_b128 v255, v[164:167] offset:20480\n\t"
    "ds_read_b32 v164, v254 offset:20480\n\t"
    "s_waitcnt lgkmcnt(15)\n\t"
    "v_smfmac_f32_16x16x64_bf16 v[44:47], a[112:115], v[84:91], v26\n\t"
    "ds_read_b32 v165, v254 offset:20544\n\t"
    "ds_read_b32 v166, v254 offset:20608\n\t"
    "ds_read_b32 v167, v254 offset:20672\n\t"
    "global_load_dwordx4 a[164:167], v11, s[30:31] offset:1024 sc1\n\t"
    "v_smfmac_f32_16x16x64_bf16 v[48:51], a[116:119], v[84:91], v26 abid:1\n\t"
    "ds_read_b64 v[76:77], v204 offset:0\n\t"
    "global_load_dwordx4 a[168:171], v11, s[30:31] offset:2048 sc1\n\t"
    "v_smfmac_f32_16x16x64_bf16 v[52:55], a[120:123], v[84:91], v27\n\t"
    "s_waitcnt vmcnt(17)\n\t"
    "ds_write_b128 v255, v[168:171] offset:21504\n\t"
    "ds_read_b32 v168, v254 offset:21504\n\t"
    "v_smfmac_f32_16x16x64_bf16 v[56:59], a[124:127], v[84:91], v27 abid:1\n\t"
    "ds_read_b64 v[78:79], v205 offset:0\n\t"
    "ds_read_b32 v169, v254 offset:21568\n\t"
    "ds_read_b64 v[80:81], v206 offset:0\n\t"
    "ds_read_b32 v170, v254 offset:21632\n\t"
    "ds_read_b64 v[82:83], v207 offset:0\n\t"
    "ds_read_b32 v171, v254 offset:21696\n\t"
    "s_waitcnt vmcnt(10)\n\t"
    "v_smfmac_f32_16x16x64_bf16 v[100:103], a[128:131], v[92:99], v28\n\t"
    "ds_read_b128 v[84:87], v2 offset:5120\n\t"
    "global_load_dwordx4 a[172:175], v11, s[30:31] offset:3072 sc1\n\t"
    "s_add_u32 s30, s30, 4096\n\t"
    "s_addc_u32 s31, s31, 0\n\t"
    "s_waitcnt vmcnt(10)\n\t"
    "v_smfmac_f32_16x16x64_bf16 v[104:107], a[132:135], v[92:99], v28 abid:1\n\t"
    "ds_read_b128 v[88:91], v3 offset:5120\n\t"
    "ds_write_b128 v255, v[172:175] offset:30720\n\t"
    "ds_read_b32 v172, v254 offset:30720\n\t"
    "s_waitcnt vmcnt(9)\n\t"
    "v_smfmac_f32_16x16x64_bf16 v[108:111], a[136:139], v[92:99], v29\n\t"
    "ds_read_b32 v173, v254 offset:30784\n\t"
    "ds_read_b32 v174, v254 offset:30848\n\t"
    "ds_read_b32 v175, v254 offset:30912\n\t"
    "s_waitcnt vmcnt(8)\n\t"
    "v_smfmac_f32_16x16x64_bf16 v[112:115], a[140:143], v[92:99], v29 abid:1\n\t"
    "ds_read_b64 v[92:93], v212 offset:0\n\t"
    "global_load_dwordx4 a[176:179], v11, s[30:31] offset:0 sc1\n\t"
    "s_waitcnt lgkmcnt(15)\n\t"
    "v_smfmac_f32_16x16x64_bf16 v[100:103], v[148:151], v[60:67], v10\n\t"
    "ds_write_b128 v255, v[176:179] offset:31744\n\t"
    "ds_read_b32 v176, v254 offset:31744\n\t"
    "v_smfmac_f32_16x16x64_bf16 v[116:119], a[128:131], v[68:75], v28\n\t"
    "ds_read_b64 v[94:95], v213 offset:0\n\t"
    "ds_read_b32 v177, v254 offset:31808\n\t"
    "ds_read_b64 v[96:97], v214 offset:0\n\t"
    "ds_read_b32 v178, v254 offset:31872\n\t"
    "ds_read_b64 v[98:99], v215 offset:0\n\t"
    "ds_read_b32 v179, v254 offset:31936\n\t"
    "ds_read_b128 v[60:63], v2 offset:7168\n\t"
    "v_smfmac_f32_16x16x64_bf16 v[120:123], a[132:135], v[68:75], v28 abid:1\n\t"
    "ds_read_b128 v[64:67], v3 offset:7168\n\t"
    "global_load_dwordx4 a[180:183], v11, s[30:31] offset:1024 sc1\n\t"
    "v_smfmac_f32_16x16x64_bf16 v[124:127], a[136:139], v[68:75], v29\n\t"
    "v_smfmac_f32_16x16x64_bf16 v[128:131], a[140:143], v[68:75], v29 abid:1\n\t"
    "ds_read_b64 v[68:69], v220 offset:0\n\t"
    "s_waitcnt lgkmcnt(15)\n\t"
    "v_smfmac_f32_16x16x64_bf16 v[104:107], v[152:155], v[76:83], v10\n\t"
    "ds_read_b64 v[70:71], v221 offset:0\n\t"
    "global_load_dwordx4 a[184:187], v11, s[30:31] offset:2048 sc1\n\t"
    "v_smfmac_f32_16x16x64_bf16 v[236:239], a[128:131], v[84:91], v28\n\t"
    "ds_read_b64 v[72:73], v222 offset:0\n\t"
    "ds_read_b64 v[74:75], v223 offset:0\n\t"
    "ds_read_b128 v[76:79], v4 offset:1024\n\t"
    "v_smfmac_f32_16x16x64_bf16 v[240:243], a[132:135], v[84:91], v28 abid:1\n\t"
    "ds_read_b128 v[80:83], v5 offset:1024\n\t"
    "v_smfmac_f32_16x16x64_bf16 v[244:247], a[136:139], v[84:91], v29\n\t"
    "global_load_dwordx4 a[188:191], v11, s[30:31] offset:3072 sc1\n\t"
    "s_add_u32 s30, s30, 4096\n\t"
    "s_addc_u32 s31, s31, 0\n\t"
    "v_smfmac_f32_16x16x64_bf16 v[248:251], a[140:143], v[84:91], v29 abid:1\n\t"
    "ds_read_b64 v[84:85], v200 offset:0\n\t"
    "s_waitcnt lgkmcnt(7)\n\t"
    "v_smfmac_f32_16x16x64_bf16 v[108:111], v[156:159], v[92:99], v10\n\t"
    "ds_read_b64 v[86:87], v201 offset:0\n\t"
    "v_smfmac_f32_16x16x64_bf16 v[44:47], a[128:131], v[60:67], v28\n\t"
    "ds_read_b64 v[88:89], v202 offset:0\n\t"
    "ds_read_b64 v[90:91], v203 offset:0\n\t"
    "ds_read_b128 v[92:95], v4 offset:3072\n\t"
    "v_smfmac_f32_16x16x64_bf16 v[48:51], a[132:135], v[60:67], v28 abid:1\n\t"
    "ds_read_b128 v[96:99], v5 offset:3072\n\t"
    "global_load_dwordx4 a[192:195], v11, s[30:31] offset:0 sc1\n\t"
    "v_smfmac_f32_16x16x64_bf16 v[52:55], a[136:139], v[60:67], v29\n\t"
    "v_smfmac_f32_16x16x64_bf16 v[56:59], a[140:143], v[60:67], v29 abid:1\n\t"
    "ds_read_b64 v[60:61], v208 offset:0\n\t"
    "global_load_dwordx4 a[196:199], v11, s[30:31] offset:1024 sc1\n\t"
    "s_waitcnt lgkmcnt(7)\n\t"
    "v_smfmac_f32_16x16x64_bf16 v[112:115], v[160:163], v[68:75], v10\n\t"
    "ds_read_b64 v[62:63], v209 offset:0\n\t"
    "s_waitcnt vmcnt(13)\n\t"
    "v_smfmac_f32_16x16x64_bf16 v[100:103], a[144:147], v[76:83], v30\n\t"
    "ds_read_b64 v[64:65], v210 offset:0\n\t"
    "ds_read_b64 v[66:67], v211 offset:0\n\t"
    "ds_read_b128 v[68:71], v4 offset:5120\n\t"
    "s_waitcnt vmcnt(12)\n\t"
    "v_smfmac_f32_16x16x64_bf16 v[104:107], a[148:151], v[76:83], v30 abid:1\n\t"
    "ds_read_b128 v[72:75], v5 offset:5120\n\t"
    "global_load_dwordx4 a[200:203], v11, s[30:31] offset:2048 sc1\n\t"
    "s_waitcnt vmcnt(12)\n\t"
    "v_smfmac_f32_16x16x64_bf16 v[108:111], a[152:155], v[76:83], v31\n\t"
    "s_waitcnt vmcnt(11)\n\t"
    "v_smfmac_f32_16x16x64_bf16 v[112:115], a[156:159], v[76:83], v31 abid:1\n\t"
    "ds_read_b64 v[76:77], v216 offset:0\n\t"
    "s_waitcnt lgkmcnt(7)\n\t"
    "v_smfmac_f32_16x16x64_bf16 v[100:103], v[164:167], v[84:91], v10\n\t"
    "ds_read_b64 v[78:79], v217 offset:0\n\t"
    "global_load_dwordx4 a[204:207], v11, s[30:31] offset:3072 sc1\n\t"
    "s_add_u32 s30, s30, 4096\n\t"
    "s_addc_u32 s31, s31, 0\n\t"
    "v_smfmac_f32_16x16x64_bf16 v[116:119], a[144:147], v[92:99], v30\n\t"
    "ds_read_b64 v[80:81], v218 offset:0\n\t"
    "ds_read_b64 v[82:83], v219 offset:0\n\t"
    "ds_read_b128 v[84:87], v2 offset:1280\n\t"
    "v_smfmac_f32_16x16x64_bf16 v[120:123], a[148:151], v[92:99], v30 abid:1\n\t"
    "ds_read_b128 v[88:91], v3 offset:1280\n\t"
    "v_smfmac_f32_16x16x64_bf16 v[124:127], a[152:155], v[92:99], v31\n\t"
    "global_load_dwordx4 a[208:211], v11, s[30:31] offset:0 sc1\n\t"
    "v_smfmac_f32_16x16x64_bf16 v[128:131], a[156:159], v[92:99], v31 abid:1\n\t"
    "ds_read_b64 v[92:93], v224 offset:0\n\t"
    "s_waitcnt lgkmcnt(7)\n\t"
    "v_smfmac_f32_16x16x64_bf16 v[104:107], v[168:171], v[60:67], v10\n\t"
    "ds_read_b64 v[94:95], v225 offset:0\n\t"
    "v_smfmac_f32_16x16x64_bf16 v[236:239], a[144:147], v[68:75], v30\n\t"
    "ds_read_b64 v[96:97], v226 offset:0\n\t"
    "ds_read_b64 v[98:99], v227 offset:0\n\t"
    "ds_read_b128 v[60:63], v2 offset:3328\n\t"
    "v_smfmac_f32_16x16x64_bf16 v[240:243], a[148:151], v[68:75], v30 abid:1\n\t"
    "ds_read_b128 v[64:67], v3 offset:3328\n\t"
    "global_load_dwordx4 a[212:215], v11, s[30:31] offset:1024 sc1\n\t"
    "v_smfmac_f32_16x16x64_bf16 v[244:247], a[152:155], v[68:75], v31\n\t"
    "v_smfmac_f32_16x16x64_bf16 v[248:251], a[156:159], v[68:75], v31 abid:1\n\t"
    "ds_read_b64 v[68:69], v196 offset:2048\n\t"
    "global_load_dwordx4 a[216:219], v11, s[30:31] offset:2048 sc1\n\t"
    "s_waitcnt lgkmcnt(7)\n\t"
    "v_smfmac_f32_16x16x64_bf16 v[108:111], v[172:175], v[76:83], v10\n\t"
    "ds_read_b64 v[70:71], v197 offset:2048\n\t"
    "s_waitcnt vmcnt(14)\n\t"
    "v_smfmac_f32_16x16x64_bf16 v[100:103], a[160:163], v[84:91], v32\n\t"
    "ds_read_b64 v[72:73], v198 offset:2048\n\t"
    "ds_read_b64 v[74:75], v199 offset:2048\n\t"
    "ds_read_b128 v[76:79], v2 offset:5376\n\t"
    "s_waitcnt vmcnt(13)\n\t"
    "v_smfmac_f32_16x16x64_bf16 v[104:107], a[164:167], v[84:91], v32 abid:1\n\t"
    "ds_read_b128 v[80:83], v3 offset:5376\n\t"
    "global_load_dwordx4 a[220:223], v11, s[30:31] offset:3072 sc1\n\t"
    "s_add_u32 s30, s30, 4096\n\t"
    "s_addc_u32 s31, s31, 0\n\t"
    "s_waitcnt vmcnt(13)\n\t"
    "v_smfmac_f32_16x16x64_bf16 v[108:111], a[168:171], v[84:91], v33\n\t"
    "s_waitcnt vmcnt(12)\n\t"
    "v_smfmac_f32_16x16x64_bf16 v[112:115], a[172:175], v[84:91], v33 abid:1\n\t"
    "ds_read_b64 v[84:85], v204 offset:2048\n\t"
    "s_waitcnt lgkmcnt(7)\n\t"
    "v_smfmac_f32_16x16x64_bf16 v[112:115], v[176:179], v[92:99], v10\n\t"
    "ds_read_b64 v[86:87], v205 offset:2048\n\t"
    "global_load_dwordx4 a[224:227], v11, s[30:31] offset:0 sc1\n\t"
    "v_smfmac_f32_16x16x64_bf16 v[116:119], a[160:163], v[60:67], v32\n\t"
    "ds_read_b64 v[88:89], v206 offset:2048\n\t"
    "ds_read_b64 v[90:91], v207 offset:2048\n\t"
    "ds_read_b128 v[92:95], v4 offset:1280\n\t"
    "v_smfmac_f32_16x16x64_bf16 v[120:123], a[164:167], v[60:67], v32 abid:1\n\t"
    "ds_read_b128 v[96:99], v5 offset:1280\n\t"
    "v_smfmac_f32_16x16x64_bf16 v[124:127], a[168:171], v[60:67], v33\n\t"
    "global_load_dwordx4 a[228:231], v11, s[30:31] offset:1024 sc1\n\t"
    "v_smfmac_f32_16x16x64_bf16 v[128:131], a[172:175], v[60:67], v33 abid:1\n\t"
    "ds_read_b64 v[60:61], v212 offset:2048\n\t"
    "s_waitcnt lgkmcnt(7)\n\t"
    "v_smfmac_f32_16x16x64_bf16 v[116:119], v[148:151], v[68:75], v10\n\t"
    "ds_read_b64 v[62:63], v213 offset:2048\n\t"
    "v_smfmac_f32_16x16x64_bf16 v[236:239], a[160:163], v[76:83], v32\n\t"
    "ds_read_b64 v[64:65], v214 offset:2048\n\t"
    "ds_read_b64 v[66:67], v215 offset:2048\n\t"
    "ds_read_b128 v[68:71], v4 offset:3328\n\t"
    "v_smfmac_f32_16x16x64_bf16 v[240:243], a[164:167], v[76:83], v32 abid:1\n\t"
    "ds_read_b128 v[72:75], v5 offset:3328\n\t"
    "global_load_dwordx4 a[232:235], v11, s[30:31] offset:2048 sc1\n\t"
    "v_smfmac_f32_16x16x64_bf16 v[244:247], a[168:171], v[76:83], v33\n\t"
    "v_smfmac_f32_16x16x64_bf16 v[248:251], a[172:175], v[76:83], v33 abid:1\n\t"
    "ds_read_b64 v[76:77], v220 offset:2048\n\t"
    "global_load_dwordx4 a[236:239], v11, s[30:31] offset:3072 sc1\n\t"
    "s_add_u32 s30, s30, 4096\n\t"
    "s_addc_u32 s31, s31, 0\n\t"
    "s_waitcnt lgkmcnt(7)\n\t"
    "v_smfmac_f32_16x16x64_bf16 v[120:123], v[152:155], v[84:91], v10\n\t"
    "ds_read_b64 v[78:79], v221 offset:2048\n\t"
    "s_waitcnt vmcnt(15)\n\t"
    "v_smfmac_f32_16x16x64_bf16 v[100:103], a[176:179], v[92:99], v34\n\t"
    "ds_read_b64 v[80:81], v222 offset:2048\n\t"
    "ds_read_b64 v[82:83], v223 offset:2048\n\t"
    "ds_read_b128 v[84:87], v4 offset:5376\n\t"
    "s_waitcnt vmcnt(14)\n\t"
    "v_smfmac_f32_16x16x64_bf16 v[104:107], a[180:183], v[92:99], v34 abid:1\n\t"
    "ds_read_b128 v[88:91], v5 offset:5376\n\t"
    "global_load_dwordx4 a[240:243], v11, s[30:31] offset:0 sc1\n\t"
    "s_waitcnt vmcnt(14)\n\t"
    "v_smfmac_f32_16x16x64_bf16 v[108:111], a[184:187], v[92:99], v35\n\t"
    "s_waitcnt vmcnt(13)\n\t"
    "v_smfmac_f32_16x16x64_bf16 v[112:115], a[188:191], v[92:99], v35 abid:1\n\t"
    "ds_read_b64 v[92:93], v200 offset:2048\n\t"
    "s_waitcnt lgkmcnt(7)\n\t"
    "v_smfmac_f32_16x16x64_bf16 v[124:127], v[156:159], v[60:67], v10\n\t"
    "ds_read_b64 v[94:95], v201 offset:2048\n\t"
    "global_load_dwordx4 a[244:247], v11, s[30:31] offset:1024 sc1\n\t"
    "v_smfmac_f32_16x16x64_bf16 v[116:119], a[176:179], v[68:75], v34\n\t"
    "ds_read_b64 v[96:97], v202 offset:2048\n\t"
    "ds_read_b64 v[98:99], v203 offset:2048\n\t"
    "ds_read_b128 v[60:63], v2 offset:1536\n\t"
    "v_smfmac_f32_16x16x64_bf16 v[120:123], a[180:183], v[68:75], v34 abid:1\n\t"
    "ds_read_b128 v[64:67], v3 offset:1536\n\t"
    "v_smfmac_f32_16x16x64_bf16 v[124:127], a[184:187], v[68:75], v35\n\t"
    "global_load_dwordx4 a[248:251], v11, s[30:31] offset:2048 sc1\n\t"
    "v_smfmac_f32_16x16x64_bf16 v[128:131], a[188:191], v[68:75], v35 abid:1\n\t"
    "ds_read_b64 v[68:69], v208 offset:2048\n\t"
    "s_waitcnt lgkmcnt(7)\n\t"
    "v_smfmac_f32_16x16x64_bf16 v[128:131], v[160:163], v[76:83], v10\n\t"
    "ds_read_b64 v[70:71], v209 offset:2048\n\t"
    "v_smfmac_f32_16x16x64_bf16 v[236:239], a[176:179], v[84:91], v34\n\t"
    "ds_read_b64 v[72:73], v210 offset:2048\n\t"
    "ds_read_b64 v[74:75], v211 offset:2048\n\t"
    "ds_read_b128 v[76:79], v2 offset:3584\n\t"
    "v_smfmac_f32_16x16x64_bf16 v[240:243], a[180:183], v[84:91], v34 abid:1\n\t"
    "ds_read_b128 v[80:83], v3 offset:3584\n\t"
    "global_load_dwordx4 a[252:255], v11, s[30:31] offset:3072 sc1\n\t"
    "s_add_u32 s30, s30, 4096\n\t"
    "s_addc_u32 s31, s31, 0\n\t"
    "v_smfmac_f32_16x16x64_bf16 v[244:247], a[184:187], v[84:91], v35\n\t"
    "v_smfmac_f32_16x16x64_bf16 v[248:251], a[188:191], v[84:91], v35 abid:1\n\t"
    "ds_read_b64 v[84:85], v216 offset:2048\n\t"
    "s_add_u32 m0, s17, 8192\n\t"
    "s_nop 0\n\t"
    "buffer_load_dwordx4 v6, s[8:11], s16 offen offset:0  lds\n\t"
    "s_waitcnt lgkmcnt(7)\n\t"
    "v_smfmac_f32_16x16x64_bf16 v[116:119], v[164:167], v[92:99], v10\n\t"
    "ds_read_b64 v[86:87], v217 offset:2048\n\t"
    "s_waitcnt vmcnt(16)\n\t"
    "v_smfmac_f32_16x16x64_bf16 v[100:103], a[192:195], v[60:67], v36\n\t"
    "ds_read_b64 v[88:89], v218 offset:2048\n\t"
    "ds_read_b64 v[90:91], v219 offset:2048\n\t"
    "ds_read_b128 v[92:95], v2 offset:5632\n\t"
    "s_waitcnt vmcnt(15)\n\t"
    "v_smfmac_f32_16x16x64_bf16 v[104:107], a[196:199], v[60:67], v36 abid:1\n\t"
    "ds_read_b128 v[96:99], v3 offset:5632\n\t"
    "buffer_load_dwordx4 v6, s[8:11], s16 offen offset:1024  lds\n\t"
    "s_waitcnt vmcnt(15)\n\t"
    "v_smfmac_f32_16x16x64_bf16 v[108:111], a[200:203], v[60:67], v37\n\t"
    "s_waitcnt vmcnt(14)\n\t"
    "v_smfmac_f32_16x16x64_bf16 v[112:115], a[204:207], v[60:67], v37 abid:1\n\t"
    "ds_read_b64 v[60:61], v224 offset:2048\n\t"
    "s_waitcnt lgkmcnt(7)\n\t"
    "v_smfmac_f32_16x16x64_bf16 v[120:123], v[168:171], v[68:75], v10\n\t"
    "ds_read_b64 v[62:63], v225 offset:2048\n\t"
    "s_add_u32 m0, s17, 18432\n\t"
    "s_nop 0\n\t"
    "buffer_load_dwordx4 v7, s[8:11], s16 offen offset:0  lds\n\t"
    "v_smfmac_f32_16x16x64_bf16 v[116:119], a[192:195], v[76:83], v36\n\t"
    "ds_read_b64 v[64:65], v226 offset:2048\n\t"
    "ds_read_b64 v[66:67], v227 offset:2048\n\t"
    "ds_read_b128 v[68:71], v4 offset:1536\n\t"
    "v_smfmac_f32_16x16x64_bf16 v[120:123], a[196:199], v[76:83], v36 abid:1\n\t"
    "ds_read_b128 v[72:75], v5 offset:1536\n\t"
    "v_smfmac_f32_16x16x64_bf16 v[124:127], a[200:203], v[76:83], v37\n\t"
    "buffer_load_dwordx4 v7, s[8:11], s16 offen offset:1024  lds\n\t"
    "v_smfmac_f32_16x16x64_bf16 v[128:131], a[204:207], v[76:83], v37 abid:1\n\t"
    "ds_read_b64 v[76:77], v196 offset:4096\n\t"
    "s_waitcnt lgkmcnt(7)\n\t"
    "v_smfmac_f32_16x16x64_bf16 v[124:127], v[172:175], v[84:91], v10\n\t"
    "ds_read_b64 v[78:79], v197 offset:4096\n\t"
    "v_smfmac_f32_16x16x64_bf16 v[236:239], a[192:195], v[92:99], v36\n\t"
    "ds_read_b64 v[80:81], v198 offset:4096\n\t"
    "ds_read_b64 v[82:83], v199 offset:4096\n\t"
    "ds_read_b64 v[84:85], v204 offset:4096\n\t"
    "v_smfmac_f32_16x16x64_bf16 v[240:243], a[196:199], v[92:99], v36 abid:1\n\t"
    "ds_read_b64 v[86:87], v205 offset:4096\n\t"
    "ds_read_b64 v[88:89], v206 offset:4096\n\t"
    "ds_read_b64 v[90:91], v207 offset:4096\n\t"
    "v_smfmac_f32_16x16x64_bf16 v[244:247], a[200:203], v[92:99], v37\n\t"
    "s_add_u32 m0, s17, 28672\n\t"
    "s_nop 0\n\t"
    "buffer_load_dwordx4 v8, s[8:11], s16 offen offset:0  lds\n\t"
    "v_smfmac_f32_16x16x64_bf16 v[248:251], a[204:207], v[92:99], v37 abid:1\n\t"
    "ds_read_b128 v[92:95], v2 offset:1792\n\t"
    "buffer_load_dwordx4 v8, s[8:11], s16 offen offset:1024  lds\n\t"
    "s_waitcnt lgkmcnt(9)\n\t"
    "v_smfmac_f32_16x16x64_bf16 v[128:131], v[176:179], v[60:67], v10\n\t"
    "ds_read_b128 v[96:99], v3 offset:1792\n\t"
    "s_waitcnt vmcnt(17)\n\t"
    "v_smfmac_f32_16x16x64_bf16 v[100:103], a[208:211], v[68:75], v38\n\t"
    "ds_read_b64 v[60:61], v212 offset:4096\n\t"
    "s_waitcnt vmcnt(16)\n\t"
    "v_smfmac_f32_16x16x64_bf16 v[104:107], a[212:215], v[68:75], v38 abid:1\n\t"
    "ds_read_b64 v[62:63], v213 offset:4096\n\t"
    "ds_read_b64 v[64:65], v214 offset:4096\n\t"
    "ds_read_b64 v[66:67], v215 offset:4096\n\t"
    "s_waitcnt vmcnt(15)\n\t"
    "v_smfmac_f32_16x16x64_bf16 v[108:111], a[216:219], v[68:75], v39\n\t"
    "s_add_u32 m0, s17, 38912\n\t"
    "s_nop 0\n\t"
    "buffer_load_dwordx4 v9, s[8:11], s16 offen offset:0  lds\n\t"
    "s_waitcnt vmcnt(15)\n\t"
    "v_smfmac_f32_16x16x64_bf16 v[112:115], a[220:223], v[68:75], v39 abid:1\n\t"
    "ds_read_b64 v[68:69], v220 offset:4096\n\t"
    "s_waitcnt lgkmcnt(7)\n\t"
    "v_smfmac_f32_16x16x64_bf16 v[236:239], v[148:151], v[76:83], v10\n\t"
    "ds_read_b64 v[70:71], v221 offset:4096\n\t"
    "buffer_load_dwordx4 v9, s[8:11], s16 offen offset:1024  lds\n\t"
    "s_add_u32 s52, s52, 32768\n\t"
    "s_xor_b32 s16, s52, s53\n\t"
    "v_smfmac_f32_16x16x64_bf16 v[240:243], v[152:155], v[84:91], v10\n\t"
    "ds_read_b64 v[72:73], v222 offset:4096\n\t"
    "ds_read_b64 v[74:75], v223 offset:4096\n\t"
    "ds_read_b128 v[76:79], v4 offset:1792\n\t"
    "s_waitcnt lgkmcnt(9)\n\t"
    "s_waitcnt vmcnt(15)\n\t"
    "v_smfmac_f32_16x16x64_bf16 v[100:103], a[224:227], v[92:99], v40\n\t"
    "ds_read_b128 v[80:83], v5 offset:1792\n\t"
    "ds_read_b128 v[84:87], v4 offset:3584\n\t"
    "s_waitcnt vmcnt(14)\n\t"
    "v_smfmac_f32_16x16x64_bf16 v[104:107], a[228:231], v[92:99], v40 abid:1\n\t"
    "ds_read_b128 v[88:91], v5 offset:3584\n\t"
    "s_waitcnt vmcnt(13)\n\t"
    "v_smfmac_f32_16x16x64_bf16 v[108:111], a[232:235], v[92:99], v41\n\t"
    "s_waitcnt vmcnt(12)\n\t"
    "v_smfmac_f32_16x16x64_bf16 v[112:115], a[236:239], v[92:99], v41 abid:1\n\t"
    "ds_read_b64 v[92:93], v200 offset:4096\n\t"
    "s_waitcnt lgkmcnt(9)\n\t"
    "v_smfmac_f32_16x16x64_bf16 v[244:247], v[156:159], v[60:67], v10\n\t"
    "ds_read_b64 v[94:95], v201 offset:4096\n\t"
    "s_waitcnt lgkmcnt(6)\n\t"
    "v_smfmac_f32_16x16x64_bf16 v[248:251], v[160:163], v[68:75], v10\n\t"
    "ds_read_b64 v[96:97], v202 offset:4096\n\t"
    "ds_read_b64 v[98:99], v203 offset:4096\n\t"
    "ds_read_b64 v[60:61], v208 offset:4096\n\t"
    "s_waitcnt lgkmcnt(7)\n\t"
    "s_waitcnt vmcnt(11)\n\t"
    "v_smfmac_f32_16x16x64_bf16 v[100:103], a[240:243], v[76:83], v42\n\t"
    "ds_read_b64 v[62:63], v209 offset:4096\n\t"
    "ds_read_b64 v[64:65], v210 offset:4096\n\t"
    "ds_read_b64 v[66:67], v211 offset:4096\n\t"
    "ds_read_b128 v[68:71], v2 offset:3840\n\t"
    "s_waitcnt vmcnt(10)\n\t"
    "v_smfmac_f32_16x16x64_bf16 v[104:107], a[244:247], v[76:83], v42 abid:1\n\t"
    "ds_read_b128 v[72:75], v3 offset:3840\n\t"
    "s_waitcnt vmcnt(9)\n\t"
    "v_smfmac_f32_16x16x64_bf16 v[108:111], a[248:251], v[76:83], v43\n\t"
    "s_waitcnt vmcnt(8)\n\t"
    "v_smfmac_f32_16x16x64_bf16 v[112:115], a[252:255], v[76:83], v43 abid:1\n\t"
    "ds_read_b64 v[76:77], v216 offset:4096\n\t"
    "s_waitcnt lgkmcnt(11)\n\t"
    "v_smfmac_f32_16x16x64_bf16 v[116:119], a[208:211], v[84:91], v38\n\t"
    "ds_read_b64 v[78:79], v217 offset:4096\n\t"
    "v_smfmac_f32_16x16x64_bf16 v[120:123], a[212:215], v[84:91], v38 abid:1\n\t"
    "ds_read_b64 v[80:81], v218 offset:4096\n\t"
    "ds_read_b64 v[82:83], v219 offset:4096\n\t"
    "v_smfmac_f32_16x16x64_bf16 v[124:127], a[216:219], v[84:91], v39\n\t"
    "v_cvt_pk_bf16_f32 v100, v100, v101\n\t"
    "v_smfmac_f32_16x16x64_bf16 v[128:131], a[220:223], v[84:91], v39 abid:1\n\t"
    "ds_read_b64 v[84:85], v224 offset:4096\n\t"
    "s_waitcnt lgkmcnt(6)\n\t"
    "v_cvt_pk_bf16_f32 v101, v102, v103\n\t"
    "v_smfmac_f32_16x16x64_bf16 v[236:239], v[164:167], v[92:99], v10\n\t"
    "ds_read_b64 v[86:87], v225 offset:4096\n\t"
    "v_cvt_pk_bf16_f32 v102, v104, v105\n\t"
    "v_smfmac_f32_16x16x64_bf16 v[240:243], v[168:171], v[60:67], v10\n\t"
    "ds_read_b64 v[88:89], v226 offset:4096\n\t"
    "ds_read_b64 v[90:91], v227 offset:4096\n\t"
    "ds_read_b128 v[92:95], v4 offset:3840\n\t"
    "s_waitcnt lgkmcnt(9)\n\t"
    "v_cvt_pk_bf16_f32 v103, v106, v107\n\t"
    "v_smfmac_f32_16x16x64_bf16 v[116:119], a[224:227], v[68:75], v40\n\t"
    "ds_read_b128 v[96:99], v5 offset:3840\n\t"
    "ds_read_b128 v[60:63], v4 offset:5632\n\t"
    "v_cvt_pk_bf16_f32 v108, v108, v109\n\t"
    "v_smfmac_f32_16x16x64_bf16 v[120:123], a[228:231], v[68:75], v40 abid:1\n\t"
    "ds_read_b128 v[64:67], v5 offset:5632\n\t"
    "v_cvt_pk_bf16_f32 v109, v110, v111\n\t"
    "v_smfmac_f32_16x16x64_bf16 v[124:127], a[232:235], v[68:75], v41\n\t"
    "v_cvt_pk_bf16_f32 v110, v112, v113\n\t"
    "v_smfmac_f32_16x16x64_bf16 v[128:131], a[236:239], v[68:75], v41 abid:1\n\t"
    "ds_read_b128 v[68:71], v2 offset:5888\n\t"
    "s_waitcnt lgkmcnt(9)\n\t"
    "v_cvt_pk_bf16_f32 v111, v114, v115\n\t"
    "v_smfmac_f32_16x16x64_bf16 v[244:247], v[172:175], v[76:83], v10\n\t"
    "ds_read_b128 v[72:75], v3 offset:5888\n\t"
    "s_waitcnt lgkmcnt(6)\n\t"
    "v_mov_b32_dpp v104, v108 row_ror:8 row_mask:0xf bank_mask:0xf\n\t"
    "v_smfmac_f32_16x16x64_bf16 v[248:251], v[176:179], v[84:91], v10\n\t"
    "ds_read_b128 v[76:79], v4 offset:5888\n\t"
    "s_waitcnt lgkmcnt(5)\n\t"
    "v_mov_b32_dpp v105, v109 row_ror:8 row_mask:0xf bank_mask:0xf\n\t"
    "v_smfmac_f32_16x16x64_bf16 v[116:119], a[240:243], v[92:99], v42\n\t"
    "ds_read_b128 v[80:83], v5 offset:5888\n\t"
    "v_mov_b32_dpp v106, v110 row_ror:8 row_mask:0xf bank_mask:0xf\n\t"
    "v_smfmac_f32_16x16x64_bf16 v[120:123], a[244:247], v[92:99], v42 abid:1\n\t"
    "ds_read_b128 v[84:87], v4 offset:7168\n\t"
    "v_mov_b32_dpp v107, v111 row_ror:8 row_mask:0xf bank_mask:0xf\n\t"
    "v_smfmac_f32_16x16x64_bf16 v[124:127], a[248:251], v[92:99], v43\n\t"
    "ds_read_b128 v[88:91], v5 offset:7168\n\t"
    "v_mov_b32_dpp v108, v100 row_ror:8 row_mask:0xf bank_mask:0x3\n\t"
    "v_smfmac_f32_16x16x64_bf16 v[128:131], a[252:255], v[92:99], v43 abid:1\n\t"
    "ds_read_b64 v[92:93], v196 offset:6144\n\t"
    "s_waitcnt lgkmcnt(7)\n\t"
    "v_mov_b32_dpp v109, v101 row_ror:8 row_mask:0xf bank_mask:0x3\n\t"
    "v_smfmac_f32_16x16x64_bf16 v[236:239], a[208:211], v[60:67], v38\n\t"
    "ds_read_b64 v[94:95], v197 offset:6144\n\t"
    "v_mov_b32_dpp v110, v102 row_ror:8 row_mask:0xf bank_mask:0x3\n\t"
    "v_smfmac_f32_16x16x64_bf16 v[240:243], a[212:215], v[60:67], v38 abid:1\n\t"
    "ds_read_b64 v[96:97], v198 offset:6144\n\t"
    "v_mov_b32_dpp v111, v103 row_ror:8 row_mask:0xf bank_mask:0x3\n\t"
    "v_smfmac_f32_16x16x64_bf16 v[244:247], a[216:219], v[60:67], v39\n\t"
    "ds_read_b64 v[98:99], v199 offset:6144\n\t"
    "v_mov_b32_dpp v100, v104 quad_perm:[0,1,2,3] row_mask:0xf bank_mask:0xc\n\t"
    "v_smfmac_f32_16x16x64_bf16 v[248:251], a[220:223], v[60:67], v39 abid:1\n\t"
    "ds_read_b128 v[60:63], v2 offset:7424\n\t"
    "s_waitcnt lgkmcnt(7)\n\t"
    "v_mov_b32_dpp v101, v105 quad_perm:[0,1,2,3] row_mask:0xf bank_mask:0xc\n\t"
    "v_smfmac_f32_16x16x64_bf16 v[236:239], a[224:227], v[68:75], v40\n\t"
    "ds_read_b128 v[64:67], v3 offset:7424\n\t"
    "v_mov_b32_dpp v102, v106 quad_perm:[0,1,2,3] row_mask:0xf bank_mask:0xc\n\t"
    "v_smfmac_f32_16x16x64_bf16 v[240:243], a[228:231], v[68:75], v40 abid:1\n\t"
    "v_mov_b32_dpp v103, v107 quad_perm:[0,1,2,3] row_mask:0xf bank_mask:0xc\n\t"
    "v_smfmac_f32_16x16x64_bf16 v[244:247], a[232:235], v[68:75], v41\n\t"
    "buffer_store_dwordx4 v[100:103], v0, s[48:51], s55 offen sc1\n\t"
    "buffer_store_dwordx4 v[108:111], v1, s[48:51], s55 offen sc1\n\t"
    "s_nop 1\n\t"
    "ds_read_b64 v[100:101], v200 offset:6144\n\t"
    "v_smfmac_f32_16x16x64_bf16 v[248:251], a[236:239], v[68:75], v41 abid:1\n\t"
    "s_waitcnt lgkmcnt(0)\n\t"
    "s_barrier\n\t"
    "ds_read_b64 v[68:69], v204 offset:6144\n\t"
    "s_add_u32 s54, s54, 32768\n\t"
    "s_xor_b32 s55, s54, s53\n\t"
    "v_cvt_pk_bf16_f32 v116, v116, v117\n\t"
    "v_cvt_pk_bf16_f32 v117, v118, v119\n\t"
    "v_cvt_pk_bf16_f32 v118, v120, v121\n\t"
    "v_smfmac_f32_16x16x64_bf16 v[236:239], a[240:243], v[76:83], v42\n\t"
    "s_add_u32 m0, s17, 0\n\t"
    "ds_read_b64 v[70:71], v205 offset:6144\n\t"
    "v_cvt_pk_bf16_f32 v119, v122, v123\n\t"
    "v_smfmac_f32_16x16x64_bf16 v[240:243], a[244:247], v[76:83], v42 abid:1\n\t"
    "buffer_load_dwordx4 v6, s[8:11], s16 offen offset:0  lds\n\t"
    "ds_read_b64 v[72:73], v206 offset:6144\n\t"
    "v_cvt_pk_bf16_f32 v124, v124, v125\n\t"
    "v_smfmac_f32_16x16x64_bf16 v[244:247], a[248:251], v[76:83], v43\n\t"
    "ds_read_b64 v[74:75], v207 offset:6144\n\t"
    "v_cvt_pk_bf16_f32 v125, v126, v127\n\t"
    "v_smfmac_f32_16x16x64_bf16 v[248:251], a[252:255], v[76:83], v43 abid:1\n\t"
    "ds_read_b128 v[76:79], v4 offset:7424\n\t"
    "buffer_load_dwordx4 v6, s[8:11], s16 offen offset:1024  lds\n\t"
    "v_cvt_pk_bf16_f32 v126, v128, v129\n\t"
    "v_smfmac_f32_16x16x64_bf16 v[44:47], a[144:147], v[84:91], v30\n\t"
    "ds_read_b128 v[80:83], v5 offset:7424\n\t"
    "s_add_u32 m0, s17, 10240\n\t"
    "v_cvt_pk_bf16_f32 v127, v130, v131\n\t"
    "v_smfmac_f32_16x16x64_bf16 v[48:51], a[148:151], v[84:91], v30 abid:1\n\t"
    "buffer_load_dwordx4 v7, s[8:11], s16 offen offset:0  lds\n\t"
    "ds_read_b64 v[102:103], v201 offset:6144\n\t"
    "v_mov_b32_dpp v120, v124 row_ror:8 row_mask:0xf bank_mask:0xf\n\t"
    "v_smfmac_f32_16x16x64_bf16 v[52:55], a[152:155], v[84:91], v31\n\t"
    "ds_read_b64 v[104:105], v202 offset:6144\n\t"
    "v_mov_b32_dpp v121, v125 row_ror:8 row_mask:0xf bank_mask:0xf\n\t"
    "v_smfmac_f32_16x16x64_bf16 v[56:59], a[156:159], v[84:91], v31 abid:1\n\t"
    "ds_read_b64 v[84:85], v212 offset:6144\n\t"
    "buffer_load_dwordx4 v7, s[8:11], s16 offen offset:1024  lds\n\t"
    "v_mov_b32_dpp v122, v126 row_ror:8 row_mask:0xf bank_mask:0xf\n\t"
    "v_smfmac_f32_16x16x64_bf16 v[44:47], v[148:151], v[92:99], v10\n\t"
    "ds_read_b64 v[86:87], v213 offset:6144\n\t"
    "s_add_u32 m0, s17, 20480\n\t"
    "v_mov_b32_dpp v123, v127 row_ror:8 row_mask:0xf bank_mask:0xf\n\t"
    "v_smfmac_f32_16x16x64_bf16 v[44:47], a[160:163], v[60:67], v32\n\t"
    "ds_read_b64 v[88:89], v214 offset:6144\n\t"
    "ds_read_b64 v[90:91], v215 offset:6144\n\t"
    "ds_read_b128 v[92:95], v2 offset:7680\n\t"
    "v_mov_b32_dpp v124, v116 row_ror:8 row_mask:0xf bank_mask:0x3\n\t"
    "v_smfmac_f32_16x16x64_bf16 v[48:51], a[164:167], v[60:67], v32 abid:1\n\t"
    "ds_read_b128 v[96:99], v3 offset:7680\n\t"
    "buffer_load_dwordx4 v8, s[8:11], s16 offen offset:0  lds\n\t"
    "v_mov_b32_dpp v125, v117 row_ror:8 row_mask:0xf bank_mask:0x3\n\t"
    "v_smfmac_f32_16x16x64_bf16 v[52:55], a[168:171], v[60:67], v33\n\t"
    "buffer_load_dwordx4 v8, s[8:11], s16 offen offset:1024  lds\n\t"
    "ds_read_b64 v[106:107], v203 offset:6144\n\t"
    "v_mov_b32_dpp v126, v118 row_ror:8 row_mask:0xf bank_mask:0x3\n\t"
    "v_smfmac_f32_16x16x64_bf16 v[56:59], a[172:175], v[60:67], v33 abid:1\n\t"
    "ds_read_b64 v[60:61], v220 offset:6144\n\t"
    "s_waitcnt lgkmcnt(9)\n\t"
    "v_mov_b32_dpp v127, v119 row_ror:8 row_mask:0xf bank_mask:0x3\n\t"
    "v_smfmac_f32_16x16x64_bf16 v[48:51], v[152:155], v[68:75], v10\n\t"
    "s_add_u32 m0, s17, 30720\n\t"
    "ds_read_b64 v[62:63], v221 offset:6144\n\t"
    "v_mov_b32_dpp v116, v120 quad_perm:[0,1,2,3] row_mask:0xf bank_mask:0xc\n\t"
    "v_smfmac_f32_16x16x64_bf16 v[44:47], a[176:179], v[76:83], v34\n\t"
    "ds_read_b64 v[64:65], v222 offset:6144\n\t"
    "ds_read_b64 v[66:67], v223 offset:6144\n\t"
    "ds_read_b128 v[68:71], v4 offset:7680\n\t"
    "buffer_load_dwordx4 v9, s[8:11], s16 offen offset:0  lds\n\t"
    "v_mov_b32_dpp v117, v121 quad_perm:[0,1,2,3] row_mask:0xf bank_mask:0xc\n\t"
    "v_smfmac_f32_16x16x64_bf16 v[48:51], a[180:183], v[76:83], v34 abid:1\n\t"
    "ds_read_b128 v[72:75], v5 offset:7680\n\t"
    "buffer_load_dwordx4 v9, s[8:11], s16 offen offset:1024  lds\n\t"
    "v_mov_b32_dpp v118, v122 quad_perm:[0,1,2,3] row_mask:0xf bank_mask:0xc\n\t"
    "v_smfmac_f32_16x16x64_bf16 v[52:55], a[184:187], v[76:83], v35\n\t"
    "ds_read_b64 v[108:109], v208 offset:6144\n\t"
    "s_add_u32 s52, s52, 32768\n\t"
    "s_xor_b32 s16, s52, s53\n\t"
    "v_mov_b32_dpp v119, v123 quad_perm:[0,1,2,3] row_mask:0xf bank_mask:0xc\n\t"
    "v_smfmac_f32_16x16x64_bf16 v[56:59], a[188:191], v[76:83], v35 abid:1\n\t"
    "buffer_store_dwordx4 v[116:119], v0, s[48:51], s55 offen sc1\n\t"
    "s_nop 1\n\t"
    "ds_read_b64 v[110:111], v209 offset:6144\n\t"
    "s_waitcnt lgkmcnt(9)\n\t"
    "v_smfmac_f32_16x16x64_bf16 v[52:55], v[156:159], v[84:91], v10\n\t"
    "ds_read_b64 v[112:113], v210 offset:6144\n\t"
    "ds_read_b64 v[114:115], v211 offset:6144\n\t"
    "ds_read_b128 v[76:79], v2 offset:7936\n\t"
    "buffer_store_dwordx4 v[124:127], v1, s[48:51], s55 offen sc1\n\t"
    "s_nop 1\n\t"
    "ds_read_b64 v[116:117], v216 offset:6144\n\t"
    "ds_read_b64 v[118:119], v217 offset:6144\n\t"
    "ds_read_b64 v[120:121], v218 offset:6144\n\t"
    "ds_read_b64 v[122:123], v219 offset:6144\n\t"
    "ds_read_b64 v[124:125], v224 offset:6144\n\t"
    "ds_read_b64 v[126:127], v225 offset:6144\n\t"
    "ds_read_b64 v[128:129], v226 offset:6144\n\t"
    "ds_read_b64 v[130:131], v227 offset:6144\n\t"
    "v_smfmac_f32_16x16x64_bf16 v[44:47], a[192:195], v[92:99], v36\n\t"
    "ds_read_b128 v[80:83], v3 offset:7936\n\t"
    "ds_read_b128 v[84:87], v4 offset:7936\n\t"
    "s_add_u32 s54, s54, 32768\n\t"
    "s_xor_b32 s55, s54, s53\n\t"
    "v_cvt_pk_bf16_f32 v236, v236, v237\n\t"
    "v_cvt_pk_bf16_f32 v237, v238, v239\n\t"
    "v_cvt_pk_bf16_f32 v238, v240, v241\n\t"
    "v_cvt_pk_bf16_f32 v239, v242, v243\n\t"
    "v_cvt_pk_bf16_f32 v244, v244, v245\n\t"
    "v_cvt_pk_bf16_f32 v245, v246, v247\n\t"
    "v_cvt_pk_bf16_f32 v246, v248, v249\n\t"
    "v_cvt_pk_bf16_f32 v247, v250, v251\n\t"
    "s_nop 1\n\t"
    "v_mov_b32_dpp v240, v244 row_ror:8 row_mask:0xf bank_mask:0xf\n\t"
    "v_mov_b32_dpp v241, v245 row_ror:8 row_mask:0xf bank_mask:0xf\n\t"
    "v_mov_b32_dpp v242, v246 row_ror:8 row_mask:0xf bank_mask:0xf\n\t"
    "v_smfmac_f32_16x16x64_bf16 v[48:51], a[196:199], v[92:99], v36 abid:1\n\t"
    "ds_read_b128 v[88:91], v5 offset:7936\n\t"
    "v_mov_b32_dpp v243, v247 row_ror:8 row_mask:0xf bank_mask:0xf\n\t"
    "v_smfmac_f32_16x16x64_bf16 v[52:55], a[200:203], v[92:99], v37\n\t"
    "v_mov_b32_dpp v244, v236 row_ror:8 row_mask:0xf bank_mask:0x3\n\t"
    "v_smfmac_f32_16x16x64_bf16 v[56:59], a[204:207], v[92:99], v37 abid:1\n\t"
    "s_waitcnt lgkmcnt(15)\n\t"
    "v_mov_b32_dpp v245, v237 row_ror:8 row_mask:0xf bank_mask:0x3\n\t"
    "v_smfmac_f32_16x16x64_bf16 v[56:59], v[160:163], v[60:67], v10\n\t"
    "v_mov_b32_dpp v246, v238 row_ror:8 row_mask:0xf bank_mask:0x3\n\t"
    "v_mov_b32_dpp v247, v239 row_ror:8 row_mask:0xf bank_mask:0x3\n\t"
    "v_mov_b32_dpp v236, v240 quad_perm:[0,1,2,3] row_mask:0xf bank_mask:0xc\n\t"
    "v_mov_b32_dpp v237, v241 quad_perm:[0,1,2,3] row_mask:0xf bank_mask:0xc\n\t"
    "v_mov_b32_dpp v238, v242 quad_perm:[0,1,2,3] row_mask:0xf bank_mask:0xc\n\t"
    "v_mov_b32_dpp v239, v243 quad_perm:[0,1,2,3] row_mask:0xf bank_mask:0xc\n\t"
    "buffer_store_dwordx4 v[236:239], v0, s[48:51], s55 offen sc1\n\t"
    "buffer_store_dwordx4 v[244:247], v1, s[48:51], s55 offen sc1\n\t"
    "s_add_u32 s54, s54, 32768\n\t"
    "s_xor_b32 s55, s54, s53\n\t"
    "s_waitcnt vmcnt(14) lgkmcnt(0)\n\t"
    "s_barrier\n\t"
    "s_mov_b32 s18, 3\n\t"
    "s_branch .LmidA_%=\n\t"
    ".LloopA_%=:\n\t"
    "ds_read_b128 v[68:71], v2 offset:2048\n\t"
    "v_smfmac_f32_16x16x64_bf16 v[44:47], a[208:211], v[84:91], v38\n\t"
    "s_add_u32 m0, s17, 6144\n\t"
    "ds_read_b128 v[72:75], v3 offset:2048\n\t"
    "v_smfmac_f32_16x16x64_bf16 v[44:47], a[224:227], v[92:99], v40\n\t"
    "buffer_load_dwordx4 v6, s[8:11], s16 offen offset:0  lds\n\t"
    "ds_read_b128 v[76:79], v4 offset:2048\n\t"
    "v_smfmac_f32_16x16x64_bf16 v[44:47], a[240:243], v[60:67], v42\n\t"
    "ds_read_b128 v[80:83], v5 offset:2048\n\t"
    "v_smfmac_f32_16x16x64_bf16 v[44:47], v[164:167], v[100:107], v10\n\t"
    "ds_read_b64 v[100:101], v196 offset:2048\n\t"
    "v_smfmac_f32_16x16x64_bf16 v[48:51], a[212:215], v[84:91], v38 abid:1\n\t"
    "ds_read_b64 v[102:103], v197 offset:2048\n\t"
    "v_smfmac_f32_16x16x64_bf16 v[48:51], a[228:231], v[92:99], v40 abid:1\n\t"
    "ds_read_b64 v[104:105], v198 offset:2048\n\t"
    "v_smfmac_f32_16x16x64_bf16 v[48:51], a[244:247], v[60:67], v42 abid:1\n\t"
    "ds_read_b64 v[106:107], v199 offset:2048\n\t"
    "v_smfmac_f32_16x16x64_bf16 v[48:51], v[168:171], v[108:115], v10\n\t"
    "ds_read_b64 v[108:109], v204 offset:2048\n\t"
    "v_cvt_pk_bf16_f32 v236, v44, v45\n\t"
    "v_smfmac_f32_16x16x64_bf16 v[52:55], a[216:219], v[84:91], v39\n\t"
    "buffer_load_dwordx4 v6, s[8:11], s16 offen offset:1024  lds\n\t"
    "ds_read_b64 v[110:111], v205 offset:2048\n\t"
    "v_cvt_pk_bf16_f32 v237, v46, v47\n\t"
    "v_smfmac_f32_16x16x64_bf16 v[52:55], a[232:235], v[92:99], v41\n\t"
    "ds_read_b64 v[112:113], v206 offset:2048\n\t"
    "v_mov_b64 v[44:45], v[132:133]\n\t"
    "v_smfmac_f32_16x16x64_bf16 v[52:55], a[248:251], v[60:67], v43\n\t"
    "ds_read_b64 v[114:115], v207 offset:2048\n\t"
    "v_mov_b64 v[46:47], v[134:135]\n\t"
    "v_smfmac_f32_16x16x64_bf16 v[52:55], v[172:175], v[116:123], v10\n\t"
    "ds_read_b64 v[116:117], v212 offset:2048\n\t"
    "v_cvt_pk_bf16_f32 v238, v48, v49\n\t"
    "v_smfmac_f32_16x16x64_bf16 v[56:59], a[220:223], v[84:91], v39 abid:1\n\t"
    "ds_read_b128 v[84:87], v2 offset:2304\n\t"
    "v_cvt_pk_bf16_f32 v239, v50, v51\n\t"
    "v_smfmac_f32_16x16x64_bf16 v[56:59], a[236:239], v[92:99], v41 abid:1\n\t"
    "ds_read_b128 v[88:91], v3 offset:2304\n\t"
    "v_mov_b64 v[48:49], v[136:137]\n\t"
    "v_smfmac_f32_16x16x64_bf16 v[56:59], a[252:255], v[60:67], v43 abid:1\n\t"
    "s_add_u32 m0, s17, 16384\n\t"
    "ds_read_b128 v[92:95], v4 offset:2304\n\t"
    "v_mov_b64 v[50:51], v[138:139]\n\t"
    "v_smfmac_f32_16x16x64_bf16 v[56:59], v[176:179], v[124:131], v10\n\t"
    "buffer_load_dwordx4 v7, s[8:11], s16 offen offset:0  lds\n\t"
    "s_waitcnt lgkmcnt(6)\n\t"
    "v_cvt_pk_bf16_f32 v240, v52, v53\n\t"
    "v_smfmac_f32_16x16x64_bf16 v[44:47], a[0:3], v[68:75], v12\n\t"
    "ds_read_b128 v[96:99], v5 offset:2304\n\t"
    "v_cvt_pk_bf16_f32 v241, v54, v55\n\t"
    "v_smfmac_f32_16x16x64_bf16 v[48:51], a[4:7], v[68:75], v12 abid:1\n\t"
    "ds_read_b64 v[118:119], v213 offset:2048\n\t"
    "v_mov_b64 v[52:53], v[140:141]\n\t"
    "v_smfmac_f32_16x16x64_bf16 v[44:47], a[16:19], v[76:83], v14\n\t"
    "ds_read_b64 v[120:121], v214 offset:2048\n\t"
    "v_mov_b64 v[54:55], v[142:143]\n\t"
    "v_smfmac_f32_16x16x64_bf16 v[48:51], a[20:23], v[76:83], v14 abid:1\n\t"
    "ds_read_b64 v[122:123], v215 offset:2048\n\t"
    "s_waitcnt lgkmcnt(5)\n\t"
    "v_cvt_pk_bf16_f32 v242, v56, v57\n\t"
    "v_smfmac_f32_16x16x64_bf16 v[44:47], a[32:35], v[84:91], v16\n\t"
    "ds_read_b64 v[124:125], v220 offset:2048\n\t"
    "v_cvt_pk_bf16_f32 v243, v58, v59\n\t"
    "v_smfmac_f32_16x16x64_bf16 v[52:55], a[8:11], v[68:75], v13\n\t"
    "ds_read_b64 v[126:127], v221 offset:2048\n\t"
    "v_mov_b64 v[56:57], v[144:145]\n\t"
    "v_smfmac_f32_16x16x64_bf16 v[48:51], a[36:39], v[84:91], v16 abid:1\n\t"
    "buffer_load_dwordx4 v7, s[8:11], s16 offen offset:1024  lds\n\t"
    "ds_read_b64 v[128:129], v222 offset:2048\n\t"
    "v_mov_b64 v[58:59], v[146:147]\n\t"
    "v_smfmac_f32_16x16x64_bf16 v[52:55], a[24:27], v[76:83], v15\n\t"
    "ds_read_b64 v[130:131], v223 offset:2048\n\t"
    "v_mov_b32_dpp v244, v240 row_ror:8 row_mask:0xf bank_mask:0xf\n\t"
    "v_smfmac_f32_16x16x64_bf16 v[52:55], a[40:43], v[84:91], v17\n\t"
    "ds_read_b128 v[60:63], v2 offset:2560\n\t"
    "v_mov_b32_dpp v245, v241 row_ror:8 row_mask:0xf bank_mask:0xf\n\t"
    "v_smfmac_f32_16x16x64_bf16 v[56:59], a[12:15], v[68:75], v13 abid:1\n\t"
    "ds_read_b128 v[64:67], v3 offset:2560\n\t"
    "v_mov_b32_dpp v246, v242 row_ror:8 row_mask:0xf bank_mask:0xf\n\t"
    "v_smfmac_f32_16x16x64_bf16 v[56:59], a[28:31], v[76:83], v15 abid:1\n\t"
    "ds_read_b128 v[68:71], v4 offset:2560\n\t"
    "v_mov_b32_dpp v247, v243 row_ror:8 row_mask:0xf bank_mask:0xf\n\t"
    "v_smfmac_f32_16x16x64_bf16 v[56:59], a[44:47], v[84:91], v17 abid:1\n\t"
    "ds_read_b128 v[72:75], v5 offset:2560\n\t"
    "s_waitcnt lgkmcnt(7)\n\t"
    "v_mov_b32_dpp v240, v236 row_ror:8 row_mask:0xf bank_mask:0x3\n\t"
    "v_smfmac_f32_16x16x64_bf16 v[44:47], a[48:51], v[92:99], v18\n\t"
    "s_add_u32 m0, s17, 26624\n\t"
    "ds_read_b128 v[76:79], v2 offset:2816\n\t"
    "v_mov_b32_dpp v241, v237 row_ror:8 row_mask:0xf bank_mask:0x3\n\t"
    "v_smfmac_f32_16x16x64_bf16 v[48:51], a[52:55], v[92:99], v18 abid:1\n\t"
    "buffer_load_dwordx4 v8, s[8:11], s16 offen offset:0  lds\n\t"
    "ds_read_b128 v[80:83], v3 offset:2816\n\t"
    "v_mov_b32_dpp v242, v238 row_ror:8 row_mask:0xf bank_mask:0x3\n\t"
    "v_smfmac_f32_16x16x64_bf16 v[52:55], a[56:59], v[92:99], v19\n\t"
    "ds_read_b128 v[84:87], v4 offset:2816\n\t"
    "v_mov_b32_dpp v243, v239 row_ror:8 row_mask:0xf bank_mask:0x3\n\t"
    "v_smfmac_f32_16x16x64_bf16 v[56:59], a[60:63], v[92:99], v19 abid:1\n\t"
    "ds_read_b128 v[88:91], v5 offset:2816\n\t"
    "v_mov_b32_dpp v236, v244 quad_perm:[0,1,2,3] row_mask:0xf bank_mask:0xc\n\t"
    "v_smfmac_f32_16x16x64_bf16 v[44:47], v[148:151], v[100:107], v10\n\t"
    "ds_read_b128 v[92:95], v2 offset:3072\n\t"
    "v_mov_b32_dpp v237, v245 quad_perm:[0,1,2,3] row_mask:0xf bank_mask:0xc\n\t"
    "v_smfmac_f32_16x16x64_bf16 v[48:51], v[152:155], v[108:115], v10\n\t"
    "ds_read_b128 v[96:99], v3 offset:3072\n\t"
    "v_mov_b32_dpp v238, v246 quad_perm:[0,1,2,3] row_mask:0xf bank_mask:0xc\n\t"
    "v_smfmac_f32_16x16x64_bf16 v[52:55], v[156:159], v[116:123], v10\n\t"
    "ds_read_b64 v[100:101], v200 offset:2048\n\t"
    "s_waitcnt lgkmcnt(7)\n\t"
    "v_mov_b32_dpp v239, v247 quad_perm:[0,1,2,3] row_mask:0xf bank_mask:0xc\n\t"
    "v_smfmac_f32_16x16x64_bf16 v[56:59], v[160:163], v[124:131], v10\n\t"
    "ds_read_b64 v[102:103], v201 offset:2048\n\t"
    "v_smfmac_f32_16x16x64_bf16 v[44:47], a[64:67], v[60:67], v20\n\t"
    "buffer_load_dwordx4 v8, s[8:11], s16 offen offset:1024  lds\n\t"
    "ds_read_b64 v[104:105], v202 offset:2048\n\t"
    "v_smfmac_f32_16x16x64_bf16 v[48:51], a[68:71], v[60:67], v20 abid:1\n\t"
    "buffer_store_dwordx4 v[236:239], v0, s[48:51], s55 offen sc1\n\t"
    "ds_read_b64 v[106:107], v203 offset:2048\n\t"
    "v_smfmac_f32_16x16x64_bf16 v[52:55], a[72:75], v[60:67], v21\n\t"
    "ds_read_b64 v[108:109], v208 offset:2048\n\t"
    "v_smfmac_f32_16x16x64_bf16 v[56:59], a[76:79], v[60:67], v21 abid:1\n\t"
    "ds_read_b128 v[60:63], v4 offset:3072\n\t"
    "v_smfmac_f32_16x16x64_bf16 v[44:47], a[80:83], v[68:75], v22\n\t"
    "ds_read_b128 v[64:67], v5 offset:3072\n\t"
    "v_smfmac_f32_16x16x64_bf16 v[48:51], a[84:87], v[68:75], v22 abid:1\n\t"
    "ds_read_b64 v[110:111], v209 offset:2048\n\t"
    "v_smfmac_f32_16x16x64_bf16 v[52:55], a[88:91], v[68:75], v23\n\t"
    "s_add_u32 m0, s17, 36864\n\t"
    "ds_read_b64 v[112:113], v210 offset:2048\n\t"
    "v_smfmac_f32_16x16x64_bf16 v[56:59], a[92:95], v[68:75], v23 abid:1\n\t"
    "buffer_load_dwordx4 v9, s[8:11], s16 offen offset:0  lds\n\t"
    "buffer_store_dwordx4 v[240:243], v1, s[48:51], s55 offen sc1\n\t"
    "s_waitcnt lgkmcnt(6)\n\t"
    "v_smfmac_f32_16x16x64_bf16 v[44:47], a[96:99], v[76:83], v24\n\t"
    "ds_read_b128 v[68:71], v2 offset:3328\n\t"
    "s_add_u32 s54, s54, 32768\n\t"
    "s_xor_b32 s55, s54, s53\n\t"
    "v_smfmac_f32_16x16x64_bf16 v[48:51], a[100:103], v[76:83], v24 abid:1\n\t"
    "ds_read_b128 v[72:75], v3 offset:3328\n\t"
    "v_smfmac_f32_16x16x64_bf16 v[52:55], a[104:107], v[76:83], v25\n\t"
    "ds_read_b64 v[114:115], v211 offset:2048\n\t"
    "v_smfmac_f32_16x16x64_bf16 v[56:59], a[108:111], v[76:83], v25 abid:1\n\t"
    "ds_read_b128 v[76:79], v4 offset:3328\n\t"
    "v_smfmac_f32_16x16x64_bf16 v[44:47], a[112:115], v[84:91], v26\n\t"
    "ds_read_b128 v[80:83], v5 offset:3328\n\t"
    "v_smfmac_f32_16x16x64_bf16 v[48:51], a[116:119], v[84:91], v26 abid:1\n\t"
    "ds_read_b64 v[116:117], v216 offset:2048\n\t"
    "v_smfmac_f32_16x16x64_bf16 v[52:55], a[120:123], v[84:91], v27\n\t"
    "buffer_load_dwordx4 v9, s[8:11], s16 offen offset:1024  lds\n\t"
    "s_add_u32 s52, s52, 32768\n\t"
    "s_xor_b32 s16, s52, s53\n\t"
    "ds_read_b64 v[118:119], v217 offset:2048\n\t"
    "v_smfmac_f32_16x16x64_bf16 v[56:59], a[124:127], v[84:91], v27 abid:1\n\t"
    "ds_read_b128 v[84:87], v2 offset:3584\n\t"
    "v_smfmac_f32_16x16x64_bf16 v[44:47], a[128:131], v[92:99], v28\n\t"
    "ds_read_b128 v[88:91], v3 offset:3584\n\t"
    "v_smfmac_f32_16x16x64_bf16 v[48:51], a[132:135], v[92:99], v28 abid:1\n\t"
    "ds_read_b64 v[120:121], v218 offset:2048\n\t"
    "v_smfmac_f32_16x16x64_bf16 v[52:55], a[136:139], v[92:99], v29\n\t"
    "ds_read_b64 v[122:123], v219 offset:2048\n\t"
    "v_smfmac_f32_16x16x64_bf16 v[56:59], a[140:143], v[92:99], v29 abid:1\n\t"
    "ds_read_b128 v[92:95], v4 offset:3584\n\t"
    "s_waitcnt lgkmcnt(7)\n\t"
    "v_smfmac_f32_16x16x64_bf16 v[44:47], a[144:147], v[60:67], v30\n\t"
    "ds_read_b128 v[96:99], v5 offset:3584\n\t"
    "v_smfmac_f32_16x16x64_bf16 v[48:51], a[148:151], v[60:67], v30 abid:1\n\t"
    "ds_read_b64 v[124:125], v224 offset:2048\n\t"
    "v_smfmac_f32_16x16x64_bf16 v[52:55], a[152:155], v[60:67], v31\n\t"
    "ds_read_b64 v[126:127], v225 offset:2048\n\t"
    "v_smfmac_f32_16x16x64_bf16 v[56:59], a[156:159], v[60:67], v31 abid:1\n\t"
    "ds_read_b128 v[60:63], v2 offset:3840\n\t"
    "v_smfmac_f32_16x16x64_bf16 v[44:47], a[160:163], v[68:75], v32\n\t"
    "ds_read_b128 v[64:67], v3 offset:3840\n\t"
    "v_smfmac_f32_16x16x64_bf16 v[48:51], a[164:167], v[68:75], v32 abid:1\n\t"
    "ds_read_b64 v[128:129], v226 offset:2048\n\t"
    "v_smfmac_f32_16x16x64_bf16 v[52:55], a[168:171], v[68:75], v33\n\t"
    "ds_read_b64 v[130:131], v227 offset:2048\n\t"
    "v_smfmac_f32_16x16x64_bf16 v[56:59], a[172:175], v[68:75], v33 abid:1\n\t"
    "ds_read_b128 v[68:71], v4 offset:3840\n\t"
    "v_smfmac_f32_16x16x64_bf16 v[44:47], a[176:179], v[76:83], v34\n\t"
    "ds_read_b128 v[72:75], v5 offset:3840\n\t"
    "v_smfmac_f32_16x16x64_bf16 v[48:51], a[180:183], v[76:83], v34 abid:1\n\t"
    "v_smfmac_f32_16x16x64_bf16 v[52:55], a[184:187], v[76:83], v35\n\t"
    "v_smfmac_f32_16x16x64_bf16 v[56:59], a[188:191], v[76:83], v35 abid:1\n\t"
    "s_waitcnt lgkmcnt(4)\n\t"
    "v_smfmac_f32_16x16x64_bf16 v[44:47], a[192:195], v[84:91], v36\n\t"
    "v_smfmac_f32_16x16x64_bf16 v[48:51], a[196:199], v[84:91], v36 abid:1\n\t"
    "v_smfmac_f32_16x16x64_bf16 v[52:55], a[200:203], v[84:91], v37\n\t"
    "v_smfmac_f32_16x16x64_bf16 v[56:59], a[204:207], v[84:91], v37 abid:1\n\t"
    "s_waitcnt vmcnt(10) lgkmcnt(0)\n\t"
    "s_barrier\n\t"
    "ds_read_b128 v[76:79], v2 offset:4096\n\t"
    "v_smfmac_f32_16x16x64_bf16 v[44:47], a[208:211], v[92:99], v38\n\t"
    "s_add_u32 m0, s17, 8192\n\t"
    "ds_read_b128 v[80:83], v3 offset:4096\n\t"
    "v_smfmac_f32_16x16x64_bf16 v[44:47], a[224:227], v[60:67], v40\n\t"
    "buffer_load_dwordx4 v6, s[8:11], s16 offen offset:0  lds\n\t"
    "ds_read_b128 v[84:87], v4 offset:4096\n\t"
    "v_smfmac_f32_16x16x64_bf16 v[44:47], a[240:243], v[68:75], v42\n\t"
    "ds_read_b128 v[88:91], v5 offset:4096\n\t"
    "v_smfmac_f32_16x16x64_bf16 v[44:47], v[164:167], v[100:107], v10\n\t"
    "ds_read_b64 v[100:101], v196 offset:4096\n\t"
    "v_smfmac_f32_16x16x64_bf16 v[48:51], a[212:215], v[92:99], v38 abid:1\n\t"
    "ds_read_b64 v[102:103], v197 offset:4096\n\t"
    "v_smfmac_f32_16x16x64_bf16 v[48:51], a[228:231], v[60:67], v40 abid:1\n\t"
    "ds_read_b64 v[104:105], v198 offset:4096\n\t"
    "v_smfmac_f32_16x16x64_bf16 v[48:51], a[244:247], v[68:75], v42 abid:1\n\t"
    "ds_read_b64 v[106:107], v199 offset:4096\n\t"
    "v_smfmac_f32_16x16x64_bf16 v[48:51], v[168:171], v[108:115], v10\n\t"
    "ds_read_b64 v[108:109], v204 offset:4096\n\t"
    "v_cvt_pk_bf16_f32 v236, v44, v45\n\t"
    "v_smfmac_f32_16x16x64_bf16 v[52:55], a[216:219], v[92:99], v39\n\t"
    "buffer_load_dwordx4 v6, s[8:11], s16 offen offset:1024  lds\n\t"
    "ds_read_b64 v[110:111], v205 offset:4096\n\t"
    "v_cvt_pk_bf16_f32 v237, v46, v47\n\t"
    "v_smfmac_f32_16x16x64_bf16 v[52:55], a[232:235], v[60:67], v41\n\t"
    "ds_read_b64 v[112:113], v206 offset:4096\n\t"
    "v_mov_b64 v[44:45], v[132:133]\n\t"
    "v_smfmac_f32_16x16x64_bf16 v[52:55], a[248:251], v[68:75], v43\n\t"
    "ds_read_b64 v[114:115], v207 offset:4096\n\t"
    "v_mov_b64 v[46:47], v[134:135]\n\t"
    "v_smfmac_f32_16x16x64_bf16 v[52:55], v[172:175], v[116:123], v10\n\t"
    "ds_read_b64 v[116:117], v212 offset:4096\n\t"
    "v_cvt_pk_bf16_f32 v238, v48, v49\n\t"
    "v_smfmac_f32_16x16x64_bf16 v[56:59], a[220:223], v[92:99], v39 abid:1\n\t"
    "ds_read_b128 v[92:95], v2 offset:4352\n\t"
    "v_cvt_pk_bf16_f32 v239, v50, v51\n\t"
    "v_smfmac_f32_16x16x64_bf16 v[56:59], a[236:239], v[60:67], v41 abid:1\n\t"
    "ds_read_b128 v[96:99], v3 offset:4352\n\t"
    "v_mov_b64 v[48:49], v[136:137]\n\t"
    "v_smfmac_f32_16x16x64_bf16 v[56:59], a[252:255], v[68:75], v43 abid:1\n\t"
    "s_add_u32 m0, s17, 18432\n\t"
    "ds_read_b128 v[60:63], v4 offset:4352\n\t"
    "v_mov_b64 v[50:51], v[138:139]\n\t"
    "v_smfmac_f32_16x16x64_bf16 v[56:59], v[176:179], v[124:131], v10\n\t"
    "buffer_load_dwordx4 v7, s[8:11], s16 offen offset:0  lds\n\t"
    "s_waitcnt lgkmcnt(6)\n\t"
    "v_cvt_pk_bf16_f32 v240, v52, v53\n\t"
    "v_smfmac_f32_16x16x64_bf16 v[44:47], a[0:3], v[76:83], v12\n\t"
    "ds_read_b128 v[64:67], v5 offset:4352\n\t"
    "v_cvt_pk_bf16_f32 v241, v54, v55\n\t"
    "v_smfmac_f32_16x16x64_bf16 v[48:51], a[4:7], v[76:83], v12 abid:1\n\t"
    "ds_read_b64 v[118:119], v213 offset:4096\n\t"
    "v_mov_b64 v[52:53], v[140:141]\n\t"
    "v_smfmac_f32_16x16x64_bf16 v[44:47], a[16:19], v[84:91], v14\n\t"
    "ds_read_b64 v[120:121], v214 offset:4096\n\t"
    "v_mov_b64 v[54:55], v[142:143]\n\t"
    "v_smfmac_f32_16x16x64_bf16 v[48:51], a[20:23], v[84:91], v14 abid:1\n\t"
    "ds_read_b64 v[122:123], v215 offset:4096\n\t"
    "s_waitcnt lgkmcnt(5)\n\t"
    "v_cvt_pk_bf16_f32 v242, v56, v57\n\t"
    "v_smfmac_f32_16x16x64_bf16 v[44:47], a[32:35], v[92:99], v16\n\t"
    "ds_read_b64 v[124:125], v220 offset:4096\n\t"
    "v_cvt_pk_bf16_f32 v243, v58, v59\n\t"
    "v_smfmac_f32_16x16x64_bf16 v[52:55], a[8:11], v[76:83], v13\n\t"
    "ds_read_b64 v[126:127], v221 offset:4096\n\t"
    "v_mov_b64 v[56:57], v[144:145]\n\t"
    "v_smfmac_f32_16x16x64_bf16 v[48:51], a[36:39], v[92:99], v16 abid:1\n\t"
    "buffer_load_dwordx4 v7, s[8:11], s16 offen offset:1024  lds\n\t"
    "ds_read_b64 v[128:129], v222 offset:4096\n\t"
    "v_mov_b64 v[58:59], v[146:147]\n\t"
    "v_smfmac_f32_16x16x64_bf16 v[52:55], a[24:27], v[84:91], v15\n\t"
    "ds_read_b64 v[130:131], v223 offset:4096\n\t"
    "v_mov_b32_dpp v244, v240 row_ror:8 row_mask:0xf bank_mask:0xf\n\t"
    "v_smfmac_f32_16x16x64_bf16 v[52:55], a[40:43], v[92:99], v17\n\t"
    "ds_read_b128 v[68:71], v2 offset:4608\n\t"
    "v_mov_b32_dpp v245, v241 row_ror:8 row_mask:0xf bank_mask:0xf\n\t"
    "v_smfmac_f32_16x16x64_bf16 v[56:59], a[12:15], v[76:83], v13 abid:1\n\t"
    "ds_read_b128 v[72:75], v3 offset:4608\n\t"
    "v_mov_b32_dpp v246, v242 row_ror:8 row_mask:0xf bank_mask:0xf\n\t"
    "v_smfmac_f32_16x16x64_bf16 v[56:59], a[28:31], v[84:91], v15 abid:1\n\t"
    "ds_read_b128 v[76:79], v4 offset:4608\n\t"
    "v_mov_b32_dpp v247, v243 row_ror:8 row_mask:0xf bank_mask:0xf\n\t"
    "v_smfmac_f32_16x16x64_bf16 v[56:59], a[44:47], v[92:99], v17 abid:1\n\t"
    "ds_read_b128 v[80:83], v5 offset:4608\n\t"
    "s_waitcnt lgkmcnt(7)\n\t"
    "v_mov_b32_dpp v240, v236 row_ror:8 row_mask:0xf bank_mask:0x3\n\t"
    "v_smfmac_f32_16x16x64_bf16 v[44:47], a[48:51], v[60:67], v18\n\t"
    "s_add_u32 m0, s17, 28672\n\t"
    "ds_read_b128 v[84:87], v2 offset:4864\n\t"
    "v_mov_b32_dpp v241, v237 row_ror:8 row_mask:0xf bank_mask:0x3\n\t"
    "v_smfmac_f32_16x16x64_bf16 v[48:51], a[52:55], v[60:67], v18 abid:1\n\t"
    "buffer_load_dwordx4 v8, s[8:11], s16 offen offset:0  lds\n\t"
    "ds_read_b128 v[88:91], v3 offset:4864\n\t"
    "v_mov_b32_dpp v242, v238 row_ror:8 row_mask:0xf bank_mask:0x3\n\t"
    "v_smfmac_f32_16x16x64_bf16 v[52:55], a[56:59], v[60:67], v19\n\t"
    "ds_read_b128 v[92:95], v4 offset:4864\n\t"
    "v_mov_b32_dpp v243, v239 row_ror:8 row_mask:0xf bank_mask:0x3\n\t"
    "v_smfmac_f32_16x16x64_bf16 v[56:59], a[60:63], v[60:67], v19 abid:1\n\t"
    "ds_read_b128 v[96:99], v5 offset:4864\n\t"
    "v_mov_b32_dpp v236, v244 quad_perm:[0,1,2,3] row_mask:0xf bank_mask:0xc\n\t"
    "v_smfmac_f32_16x16x64_bf16 v[44:47], v[148:151], v[100:107], v10\n\t"
    "ds_read_b128 v[60:63], v2 offset:5120\n\t"
    "v_mov_b32_dpp v237, v245 quad_perm:[0,1,2,3] row_mask:0xf bank_mask:0xc\n\t"
    "v_smfmac_f32_16x16x64_bf16 v[48:51], v[152:155], v[108:115], v10\n\t"
    "ds_read_b128 v[64:67], v3 offset:5120\n\t"
    "v_mov_b32_dpp v238, v246 quad_perm:[0,1,2,3] row_mask:0xf bank_mask:0xc\n\t"
    "v_smfmac_f32_16x16x64_bf16 v[52:55], v[156:159], v[116:123], v10\n\t"
    "ds_read_b64 v[100:101], v200 offset:4096\n\t"
    "s_waitcnt lgkmcnt(7)\n\t"
    "v_mov_b32_dpp v239, v247 quad_perm:[0,1,2,3] row_mask:0xf bank_mask:0xc\n\t"
    "v_smfmac_f32_16x16x64_bf16 v[56:59], v[160:163], v[124:131], v10\n\t"
    "ds_read_b64 v[102:103], v201 offset:4096\n\t"
    "v_smfmac_f32_16x16x64_bf16 v[44:47], a[64:67], v[68:75], v20\n\t"
    "buffer_load_dwordx4 v8, s[8:11], s16 offen offset:1024  lds\n\t"
    "ds_read_b64 v[104:105], v202 offset:4096\n\t"
    "v_smfmac_f32_16x16x64_bf16 v[48:51], a[68:71], v[68:75], v20 abid:1\n\t"
    "buffer_store_dwordx4 v[236:239], v0, s[48:51], s55 offen sc1\n\t"
    "ds_read_b64 v[106:107], v203 offset:4096\n\t"
    "v_smfmac_f32_16x16x64_bf16 v[52:55], a[72:75], v[68:75], v21\n\t"
    "ds_read_b64 v[108:109], v208 offset:4096\n\t"
    "v_smfmac_f32_16x16x64_bf16 v[56:59], a[76:79], v[68:75], v21 abid:1\n\t"
    "ds_read_b128 v[68:71], v4 offset:5120\n\t"
    "v_smfmac_f32_16x16x64_bf16 v[44:47], a[80:83], v[76:83], v22\n\t"
    "ds_read_b128 v[72:75], v5 offset:5120\n\t"
    "v_smfmac_f32_16x16x64_bf16 v[48:51], a[84:87], v[76:83], v22 abid:1\n\t"
    "ds_read_b64 v[110:111], v209 offset:4096\n\t"
    "v_smfmac_f32_16x16x64_bf16 v[52:55], a[88:91], v[76:83], v23\n\t"
    "s_add_u32 m0, s17, 38912\n\t"
    "ds_read_b64 v[112:113], v210 offset:4096\n\t"
    "v_smfmac_f32_16x16x64_bf16 v[56:59], a[92:95], v[76:83], v23 abid:1\n\t"
    "buffer_load_dwordx4 v9, s[8:11], s16 offen offset:0  lds\n\t"
    "buffer_store_dwordx4 v[240:243], v1, s[48:51], s55 offen sc1\n\t"
    "s_waitcnt lgkmcnt(6)\n\t"
    "v_smfmac_f32_16x16x64_bf16 v[44:47], a[96:99], v[84:91], v24\n\t"
    "ds_read_b128 v[76:79], v2 offset:5376\n\t"
    "s_add_u32 s54, s54, 32768\n\t"
    "s_xor_b32 s55, s54, s53\n\t"
    "v_smfmac_f32_16x16x64_bf16 v[48:51], a[100:103], v[84:91], v24 abid:1\n\t"
    "ds_read_b128 v[80:83], v3 offset:5376\n\t"
    "v_smfmac_f32_16x16x64_bf16 v[52:55], a[104:107], v[84:91], v25\n\t"
    "ds_read_b64 v[114:115], v211 offset:4096\n\t"
    "v_smfmac_f32_16x16x64_bf16 v[56:59], a[108:111], v[84:91], v25 abid:1\n\t"
    "ds_read_b128 v[84:87], v4 offset:5376\n\t"
    "v_smfmac_f32_16x16x64_bf16 v[44:47], a[112:115], v[92:99], v26\n\t"
    "ds_read_b128 v[88:91], v5 offset:5376\n\t"
    "v_smfmac_f32_16x16x64_bf16 v[48:51], a[116:119], v[92:99], v26 abid:1\n\t"
    "ds_read_b64 v[116:117], v216 offset:4096\n\t"
    "v_smfmac_f32_16x16x64_bf16 v[52:55], a[120:123], v[92:99], v27\n\t"
    "buffer_load_dwordx4 v9, s[8:11], s16 offen offset:1024  lds\n\t"
    "s_add_u32 s52, s52, 32768\n\t"
    "s_xor_b32 s16, s52, s53\n\t"
    "ds_read_b64 v[118:119], v217 offset:4096\n\t"
    "v_smfmac_f32_16x16x64_bf16 v[56:59], a[124:127], v[92:99], v27 abid:1\n\t"
    "ds_read_b128 v[92:95], v2 offset:5632\n\t"
    "v_smfmac_f32_16x16x64_bf16 v[44:47], a[128:131], v[60:67], v28\n\t"
    "ds_read_b128 v[96:99], v3 offset:5632\n\t"
    "v_smfmac_f32_16x16x64_bf16 v[48:51], a[132:135], v[60:67], v28 abid:1\n\t"
    "ds_read_b64 v[120:121], v218 offset:4096\n\t"
    "v_smfmac_f32_16x16x64_bf16 v[52:55], a[136:139], v[60:67], v29\n\t"
    "ds_read_b64 v[122:123], v219 offset:4096\n\t"
    "v_smfmac_f32_16x16x64_bf16 v[56:59], a[140:143], v[60:67], v29 abid:1\n\t"
    "ds_read_b128 v[60:63], v4 offset:5632\n\t"
    "s_waitcnt lgkmcnt(7)\n\t"
    "v_smfmac_f32_16x16x64_bf16 v[44:47], a[144:147], v[68:75], v30\n\t"
    "ds_read_b128 v[64:67], v5 offset:5632\n\t"
    "v_smfmac_f32_16x16x64_bf16 v[48:51], a[148:151], v[68:75], v30 abid:1\n\t"
    "ds_read_b64 v[124:125], v224 offset:4096\n\t"
    "v_smfmac_f32_16x16x64_bf16 v[52:55], a[152:155], v[68:75], v31\n\t"
    "ds_read_b64 v[126:127], v225 offset:4096\n\t"
    "v_smfmac_f32_16x16x64_bf16 v[56:59], a[156:159], v[68:75], v31 abid:1\n\t"
    "ds_read_b128 v[68:71], v2 offset:5888\n\t"
    "v_smfmac_f32_16x16x64_bf16 v[44:47], a[160:163], v[76:83], v32\n\t"
    "ds_read_b128 v[72:75], v3 offset:5888\n\t"
    "v_smfmac_f32_16x16x64_bf16 v[48:51], a[164:167], v[76:83], v32 abid:1\n\t"
    "ds_read_b64 v[128:129], v226 offset:4096\n\t"
    "v_smfmac_f32_16x16x64_bf16 v[52:55], a[168:171], v[76:83], v33\n\t"
    "ds_read_b64 v[130:131], v227 offset:4096\n\t"
    "v_smfmac_f32_16x16x64_bf16 v[56:59], a[172:175], v[76:83], v33 abid:1\n\t"
    "ds_read_b128 v[76:79], v4 offset:5888\n\t"
    "v_smfmac_f32_16x16x64_bf16 v[44:47], a[176:179], v[84:91], v34\n\t"
    "ds_read_b128 v[80:83], v5 offset:5888\n\t"
    "v_smfmac_f32_16x16x64_bf16 v[48:51], a[180:183], v[84:91], v34 abid:1\n\t"
    "v_smfmac_f32_16x16x64_bf16 v[52:55], a[184:187], v[84:91], v35\n\t"
    "v_smfmac_f32_16x16x64_bf16 v[56:59], a[188:191], v[84:91], v35 abid:1\n\t"
    "s_waitcnt lgkmcnt(4)\n\t"
    "v_smfmac_f32_16x16x64_bf16 v[44:47], a[192:195], v[92:99], v36\n\t"
    "v_smfmac_f32_16x16x64_bf16 v[48:51], a[196:199], v[92:99], v36 abid:1\n\t"
    "v_smfmac_f32_16x16x64_bf16 v[52:55], a[200:203], v[92:99], v37\n\t"
    "v_smfmac_f32_16x16x64_bf16 v[56:59], a[204:207], v[92:99], v37 abid:1\n\t"
    "s_waitcnt vmcnt(10) lgkmcnt(0)\n\t"
    "s_barrier\n\t"
    "ds_read_b128 v[84:87], v2 offset:6144\n\t"
    "v_smfmac_f32_16x16x64_bf16 v[44:47], a[208:211], v[60:67], v38\n\t"
    "s_add_u32 m0, s17, 0\n\t"
    "ds_read_b128 v[88:91], v3 offset:6144\n\t"
    "v_smfmac_f32_16x16x64_bf16 v[44:47], a[224:227], v[68:75], v40\n\t"
    "buffer_load_dwordx4 v6, s[8:11], s16 offen offset:0  lds\n\t"
    "ds_read_b128 v[92:95], v4 offset:6144\n\t"
    "v_smfmac_f32_16x16x64_bf16 v[44:47], a[240:243], v[76:83], v42\n\t"
    "ds_read_b128 v[96:99], v5 offset:6144\n\t"
    "v_smfmac_f32_16x16x64_bf16 v[44:47], v[164:167], v[100:107], v10\n\t"
    "ds_read_b64 v[100:101], v196 offset:6144\n\t"
    "v_smfmac_f32_16x16x64_bf16 v[48:51], a[212:215], v[60:67], v38 abid:1\n\t"
    "ds_read_b64 v[102:103], v197 offset:6144\n\t"
    "v_smfmac_f32_16x16x64_bf16 v[48:51], a[228:231], v[68:75], v40 abid:1\n\t"
    "ds_read_b64 v[104:105], v198 offset:6144\n\t"
    "v_smfmac_f32_16x16x64_bf16 v[48:51], a[244:247], v[76:83], v42 abid:1\n\t"
    "ds_read_b64 v[106:107], v199 offset:6144\n\t"
    "v_smfmac_f32_16x16x64_bf16 v[48:51], v[168:171], v[108:115], v10\n\t"
    "ds_read_b64 v[108:109], v204 offset:6144\n\t"
    "v_cvt_pk_bf16_f32 v236, v44, v45\n\t"
    "v_smfmac_f32_16x16x64_bf16 v[52:55], a[216:219], v[60:67], v39\n\t"
    "buffer_load_dwordx4 v6, s[8:11], s16 offen offset:1024  lds\n\t"
    "ds_read_b64 v[110:111], v205 offset:6144\n\t"
    "v_cvt_pk_bf16_f32 v237, v46, v47\n\t"
    "v_smfmac_f32_16x16x64_bf16 v[52:55], a[232:235], v[68:75], v41\n\t"
    "ds_read_b64 v[112:113], v206 offset:6144\n\t"
    "v_mov_b64 v[44:45], v[132:133]\n\t"
    "v_smfmac_f32_16x16x64_bf16 v[52:55], a[248:251], v[76:83], v43\n\t"
    "ds_read_b64 v[114:115], v207 offset:6144\n\t"
    "v_mov_b64 v[46:47], v[134:135]\n\t"
    "v_smfmac_f32_16x16x64_bf16 v[52:55], v[172:175], v[116:123], v10\n\t"
    "ds_read_b64 v[116:117], v212 offset:6144\n\t"
    "v_cvt_pk_bf16_f32 v238, v48, v49\n\t"
    "v_smfmac_f32_16x16x64_bf16 v[56:59], a[220:223], v[60:67], v39 abid:1\n\t"
    "ds_read_b128 v[60:63], v2 offset:6400\n\t"
    "v_cvt_pk_bf16_f32 v239, v50, v51\n\t"
    "v_smfmac_f32_16x16x64_bf16 v[56:59], a[236:239], v[68:75], v41 abid:1\n\t"
    "ds_read_b128 v[64:67], v3 offset:6400\n\t"
    "v_mov_b64 v[48:49], v[136:137]\n\t"
    "v_smfmac_f32_16x16x64_bf16 v[56:59], a[252:255], v[76:83], v43 abid:1\n\t"
    "s_add_u32 m0, s17, 10240\n\t"
    "ds_read_b128 v[68:71], v4 offset:6400\n\t"
    "v_mov_b64 v[50:51], v[138:139]\n\t"
    "v_smfmac_f32_16x16x64_bf16 v[56:59], v[176:179], v[124:131], v10\n\t"
    "buffer_load_dwordx4 v7, s[8:11], s16 offen offset:0  lds\n\t"
    "s_waitcnt lgkmcnt(6)\n\t"
    "v_cvt_pk_bf16_f32 v240, v52, v53\n\t"
    "v_smfmac_f32_16x16x64_bf16 v[44:47], a[0:3], v[84:91], v12\n\t"
    "ds_read_b128 v[72:75], v5 offset:6400\n\t"
    "v_cvt_pk_bf16_f32 v241, v54, v55\n\t"
    "v_smfmac_f32_16x16x64_bf16 v[48:51], a[4:7], v[84:91], v12 abid:1\n\t"
    "ds_read_b64 v[118:119], v213 offset:6144\n\t"
    "v_mov_b64 v[52:53], v[140:141]\n\t"
    "v_smfmac_f32_16x16x64_bf16 v[44:47], a[16:19], v[92:99], v14\n\t"
    "ds_read_b64 v[120:121], v214 offset:6144\n\t"
    "v_mov_b64 v[54:55], v[142:143]\n\t"
    "v_smfmac_f32_16x16x64_bf16 v[48:51], a[20:23], v[92:99], v14 abid:1\n\t"
    "ds_read_b64 v[122:123], v215 offset:6144\n\t"
    "s_waitcnt lgkmcnt(5)\n\t"
    "v_cvt_pk_bf16_f32 v242, v56, v57\n\t"
    "v_smfmac_f32_16x16x64_bf16 v[44:47], a[32:35], v[60:67], v16\n\t"
    "ds_read_b64 v[124:125], v220 offset:6144\n\t"
    "v_cvt_pk_bf16_f32 v243, v58, v59\n\t"
    "v_smfmac_f32_16x16x64_bf16 v[52:55], a[8:11], v[84:91], v13\n\t"
    "ds_read_b64 v[126:127], v221 offset:6144\n\t"
    "v_mov_b64 v[56:57], v[144:145]\n\t"
    "v_smfmac_f32_16x16x64_bf16 v[48:51], a[36:39], v[60:67], v16 abid:1\n\t"
    "buffer_load_dwordx4 v7, s[8:11], s16 offen offset:1024  lds\n\t"
    "ds_read_b64 v[128:129], v222 offset:6144\n\t"
    "v_mov_b64 v[58:59], v[146:147]\n\t"
    "v_smfmac_f32_16x16x64_bf16 v[52:55], a[24:27], v[92:99], v15\n\t"
    "ds_read_b64 v[130:131], v223 offset:6144\n\t"
    "v_mov_b32_dpp v244, v240 row_ror:8 row_mask:0xf bank_mask:0xf\n\t"
    "v_smfmac_f32_16x16x64_bf16 v[52:55], a[40:43], v[60:67], v17\n\t"
    "ds_read_b128 v[76:79], v2 offset:6656\n\t"
    "v_mov_b32_dpp v245, v241 row_ror:8 row_mask:0xf bank_mask:0xf\n\t"
    "v_smfmac_f32_16x16x64_bf16 v[56:59], a[12:15], v[84:91], v13 abid:1\n\t"
    "ds_read_b128 v[80:83], v3 offset:6656\n\t"
    "v_mov_b32_dpp v246, v242 row_ror:8 row_mask:0xf bank_mask:0xf\n\t"
    "v_smfmac_f32_16x16x64_bf16 v[56:59], a[28:31], v[92:99], v15 abid:1\n\t"
    "ds_read_b128 v[84:87], v4 offset:6656\n\t"
    "v_mov_b32_dpp v247, v243 row_ror:8 row_mask:0xf bank_mask:0xf\n\t"
    "v_smfmac_f32_16x16x64_bf16 v[56:59], a[44:47], v[60:67], v17 abid:1\n\t"
    "ds_read_b128 v[88:91], v5 offset:6656\n\t"
    "s_waitcnt lgkmcnt(7)\n\t"
    "v_mov_b32_dpp v240, v236 row_ror:8 row_mask:0xf bank_mask:0x3\n\t"
    "v_smfmac_f32_16x16x64_bf16 v[44:47], a[48:51], v[68:75], v18\n\t"
    "s_add_u32 m0, s17, 20480\n\t"
    "ds_read_b128 v[92:95], v2 offset:6912\n\t"
    "v_mov_b32_dpp v241, v237 row_ror:8 row_mask:0xf bank_mask:0x3\n\t"
    "v_smfmac_f32_16x16x64_bf16 v[48:51], a[52:55], v[68:75], v18 abid:1\n\t"
    "buffer_load_dwordx4 v8, s[8:11], s16 offen offset:0  lds\n\t"
    "ds_read_b128 v[96:99], v3 offset:6912\n\t"
    "v_mov_b32_dpp v242, v238 row_ror:8 row_mask:0xf bank_mask:0x3\n\t"
    "v_smfmac_f32_16x16x64_bf16 v[52:55], a[56:59], v[68:75], v19\n\t"
    "ds_read_b128 v[60:63], v4 offset:6912\n\t"
    "v_mov_b32_dpp v243, v239 row_ror:8 row_mask:0xf bank_mask:0x3\n\t"
    "v_smfmac_f32_16x16x64_bf16 v[56:59], a[60:63], v[68:75], v19 abid:1\n\t"
    "ds_read_b128 v[64:67], v5 offset:6912\n\t"
    "v_mov_b32_dpp v236, v244 quad_perm:[0,1,2,3] row_mask:0xf bank_mask:0xc\n\t"
    "v_smfmac_f32_16x16x64_bf16 v[44:47], v[148:151], v[100:107], v10\n\t"
    "ds_read_b128 v[68:71], v2 offset:7168\n\t"
    "v_mov_b32_dpp v237, v245 quad_perm:[0,1,2,3] row_mask:0xf bank_mask:0xc\n\t"
    "v_smfmac_f32_16x16x64_bf16 v[48:51], v[152:155], v[108:115], v10\n\t"
    "ds_read_b128 v[72:75], v3 offset:7168\n\t"
    "v_mov_b32_dpp v238, v246 quad_perm:[0,1,2,3] row_mask:0xf bank_mask:0xc\n\t"
    "v_smfmac_f32_16x16x64_bf16 v[52:55], v[156:159], v[116:123], v10\n\t"
    "ds_read_b64 v[100:101], v200 offset:6144\n\t"
    "s_waitcnt lgkmcnt(7)\n\t"
    "v_mov_b32_dpp v239, v247 quad_perm:[0,1,2,3] row_mask:0xf bank_mask:0xc\n\t"
    "v_smfmac_f32_16x16x64_bf16 v[56:59], v[160:163], v[124:131], v10\n\t"
    "ds_read_b64 v[102:103], v201 offset:6144\n\t"
    "v_smfmac_f32_16x16x64_bf16 v[44:47], a[64:67], v[76:83], v20\n\t"
    "buffer_load_dwordx4 v8, s[8:11], s16 offen offset:1024  lds\n\t"
    "ds_read_b64 v[104:105], v202 offset:6144\n\t"
    "v_smfmac_f32_16x16x64_bf16 v[48:51], a[68:71], v[76:83], v20 abid:1\n\t"
    "buffer_store_dwordx4 v[236:239], v0, s[48:51], s55 offen sc1\n\t"
    "ds_read_b64 v[106:107], v203 offset:6144\n\t"
    "v_smfmac_f32_16x16x64_bf16 v[52:55], a[72:75], v[76:83], v21\n\t"
    "ds_read_b64 v[108:109], v208 offset:6144\n\t"
    "v_smfmac_f32_16x16x64_bf16 v[56:59], a[76:79], v[76:83], v21 abid:1\n\t"
    "ds_read_b128 v[76:79], v4 offset:7168\n\t"
    "v_smfmac_f32_16x16x64_bf16 v[44:47], a[80:83], v[84:91], v22\n\t"
    "ds_read_b128 v[80:83], v5 offset:7168\n\t"
    "v_smfmac_f32_16x16x64_bf16 v[48:51], a[84:87], v[84:91], v22 abid:1\n\t"
    "ds_read_b64 v[110:111], v209 offset:6144\n\t"
    "v_smfmac_f32_16x16x64_bf16 v[52:55], a[88:91], v[84:91], v23\n\t"
    "s_add_u32 m0, s17, 30720\n\t"
    "ds_read_b64 v[112:113], v210 offset:6144\n\t"
    "v_smfmac_f32_16x16x64_bf16 v[56:59], a[92:95], v[84:91], v23 abid:1\n\t"
    "buffer_load_dwordx4 v9, s[8:11], s16 offen offset:0  lds\n\t"
    "buffer_store_dwordx4 v[240:243], v1, s[48:51], s55 offen sc1\n\t"
    "s_waitcnt lgkmcnt(6)\n\t"
    "v_smfmac_f32_16x16x64_bf16 v[44:47], a[96:99], v[92:99], v24\n\t"
    "ds_read_b128 v[84:87], v2 offset:7424\n\t"
    "s_add_u32 s54, s54, 32768\n\t"
    "s_xor_b32 s55, s54, s53\n\t"
    "v_smfmac_f32_16x16x64_bf16 v[48:51], a[100:103], v[92:99], v24 abid:1\n\t"
    "ds_read_b128 v[88:91], v3 offset:7424\n\t"
    "v_smfmac_f32_16x16x64_bf16 v[52:55], a[104:107], v[92:99], v25\n\t"
    "ds_read_b64 v[114:115], v211 offset:6144\n\t"
    "v_smfmac_f32_16x16x64_bf16 v[56:59], a[108:111], v[92:99], v25 abid:1\n\t"
    "ds_read_b128 v[92:95], v4 offset:7424\n\t"
    "v_smfmac_f32_16x16x64_bf16 v[44:47], a[112:115], v[60:67], v26\n\t"
    "ds_read_b128 v[96:99], v5 offset:7424\n\t"
    "v_smfmac_f32_16x16x64_bf16 v[48:51], a[116:119], v[60:67], v26 abid:1\n\t"
    "ds_read_b64 v[116:117], v216 offset:6144\n\t"
    "v_smfmac_f32_16x16x64_bf16 v[52:55], a[120:123], v[60:67], v27\n\t"
    "buffer_load_dwordx4 v9, s[8:11], s16 offen offset:1024  lds\n\t"
    "s_add_u32 s52, s52, 32768\n\t"
    "s_xor_b32 s16, s52, s53\n\t"
    "ds_read_b64 v[118:119], v217 offset:6144\n\t"
    "v_smfmac_f32_16x16x64_bf16 v[56:59], a[124:127], v[60:67], v27 abid:1\n\t"
    "ds_read_b128 v[60:63], v2 offset:7680\n\t"
    "v_smfmac_f32_16x16x64_bf16 v[44:47], a[128:131], v[68:75], v28\n\t"
    "ds_read_b128 v[64:67], v3 offset:7680\n\t"
    "v_smfmac_f32_16x16x64_bf16 v[48:51], a[132:135], v[68:75], v28 abid:1\n\t"
    "ds_read_b64 v[120:121], v218 offset:6144\n\t"
    "v_smfmac_f32_16x16x64_bf16 v[52:55], a[136:139], v[68:75], v29\n\t"
    "ds_read_b64 v[122:123], v219 offset:6144\n\t"
    "v_smfmac_f32_16x16x64_bf16 v[56:59], a[140:143], v[68:75], v29 abid:1\n\t"
    "ds_read_b128 v[68:71], v4 offset:7680\n\t"
    "s_waitcnt lgkmcnt(7)\n\t"
    "v_smfmac_f32_16x16x64_bf16 v[44:47], a[144:147], v[76:83], v30\n\t"
    "ds_read_b128 v[72:75], v5 offset:7680\n\t"
    "v_smfmac_f32_16x16x64_bf16 v[48:51], a[148:151], v[76:83], v30 abid:1\n\t"
    "ds_read_b64 v[124:125], v224 offset:6144\n\t"
    "v_smfmac_f32_16x16x64_bf16 v[52:55], a[152:155], v[76:83], v31\n\t"
    "ds_read_b64 v[126:127], v225 offset:6144\n\t"
    "v_smfmac_f32_16x16x64_bf16 v[56:59], a[156:159], v[76:83], v31 abid:1\n\t"
    "ds_read_b128 v[76:79], v2 offset:7936\n\t"
    "v_smfmac_f32_16x16x64_bf16 v[44:47], a[160:163], v[84:91], v32\n\t"
    "ds_read_b128 v[80:83], v3 offset:7936\n\t"
    "v_smfmac_f32_16x16x64_bf16 v[48:51], a[164:167], v[84:91], v32 abid:1\n\t"
    "ds_read_b64 v[128:129], v226 offset:6144\n\t"
    "v_smfmac_f32_16x16x64_bf16 v[52:55], a[168:171], v[84:91], v33\n\t"
    "ds_read_b64 v[130:131], v227 offset:6144\n\t"
    "v_smfmac_f32_16x16x64_bf16 v[56:59], a[172:175], v[84:91], v33 abid:1\n\t"
    "ds_read_b128 v[84:87], v4 offset:7936\n\t"
    "v_smfmac_f32_16x16x64_bf16 v[44:47], a[176:179], v[92:99], v34\n\t"
    "ds_read_b128 v[88:91], v5 offset:7936\n\t"
    "v_smfmac_f32_16x16x64_bf16 v[48:51], a[180:183], v[92:99], v34 abid:1\n\t"
    "v_smfmac_f32_16x16x64_bf16 v[52:55], a[184:187], v[92:99], v35\n\t"
    "v_smfmac_f32_16x16x64_bf16 v[56:59], a[188:191], v[92:99], v35 abid:1\n\t"
    "s_waitcnt lgkmcnt(4)\n\t"
    "v_smfmac_f32_16x16x64_bf16 v[44:47], a[192:195], v[60:67], v36\n\t"
    "v_smfmac_f32_16x16x64_bf16 v[48:51], a[196:199], v[60:67], v36 abid:1\n\t"
    "v_smfmac_f32_16x16x64_bf16 v[52:55], a[200:203], v[60:67], v37\n\t"
    "v_smfmac_f32_16x16x64_bf16 v[56:59], a[204:207], v[60:67], v37 abid:1\n\t"
    "s_waitcnt vmcnt(10) lgkmcnt(0)\n\t"
    "s_barrier\n\t"
    ".LmidA_%=:\n\t"
    "ds_read_b128 v[92:95], v2 offset:8192\n\t"
    "v_smfmac_f32_16x16x64_bf16 v[44:47], a[208:211], v[68:75], v38\n\t"
    "s_add_u32 m0, s17, 2048\n\t"
    "ds_read_b128 v[96:99], v3 offset:8192\n\t"
    "v_smfmac_f32_16x16x64_bf16 v[44:47], a[224:227], v[76:83], v40\n\t"
    "buffer_load_dwordx4 v6, s[8:11], s16 offen offset:0  lds\n\t"
    "ds_read_b128 v[60:63], v4 offset:8192\n\t"
    "v_smfmac_f32_16x16x64_bf16 v[44:47], a[240:243], v[84:91], v42\n\t"
    "ds_read_b128 v[64:67], v5 offset:8192\n\t"
    "v_smfmac_f32_16x16x64_bf16 v[44:47], v[164:167], v[100:107], v10\n\t"
    "ds_read_b64 v[100:101], v196 offset:8192\n\t"
    "v_smfmac_f32_16x16x64_bf16 v[48:51], a[212:215], v[68:75], v38 abid:1\n\t"
    "ds_read_b64 v[102:103], v197 offset:8192\n\t"
    "v_smfmac_f32_16x16x64_bf16 v[48:51], a[228:231], v[76:83], v40 abid:1\n\t"
    "ds_read_b64 v[104:105], v198 offset:8192\n\t"
    "v_smfmac_f32_16x16x64_bf16 v[48:51], a[244:247], v[84:91], v42 abid:1\n\t"
    "ds_read_b64 v[106:107], v199 offset:8192\n\t"
    "v_smfmac_f32_16x16x64_bf16 v[48:51], v[168:171], v[108:115], v10\n\t"
    "ds_read_b64 v[108:109], v204 offset:8192\n\t"
    "v_cvt_pk_bf16_f32 v236, v44, v45\n\t"
    "v_smfmac_f32_16x16x64_bf16 v[52:55], a[216:219], v[68:75], v39\n\t"
    "buffer_load_dwordx4 v6, s[8:11], s16 offen offset:1024  lds\n\t"
    "ds_read_b64 v[110:111], v205 offset:8192\n\t"
    "v_cvt_pk_bf16_f32 v237, v46, v47\n\t"
    "v_smfmac_f32_16x16x64_bf16 v[52:55], a[232:235], v[76:83], v41\n\t"
    "ds_read_b64 v[112:113], v206 offset:8192\n\t"
    "v_mov_b64 v[44:45], v[132:133]\n\t"
    "v_smfmac_f32_16x16x64_bf16 v[52:55], a[248:251], v[84:91], v43\n\t"
    "ds_read_b64 v[114:115], v207 offset:8192\n\t"
    "v_mov_b64 v[46:47], v[134:135]\n\t"
    "v_smfmac_f32_16x16x64_bf16 v[52:55], v[172:175], v[116:123], v10\n\t"
    "ds_read_b64 v[116:117], v212 offset:8192\n\t"
    "v_cvt_pk_bf16_f32 v238, v48, v49\n\t"
    "v_smfmac_f32_16x16x64_bf16 v[56:59], a[220:223], v[68:75], v39 abid:1\n\t"
    "ds_read_b128 v[68:71], v2 offset:8448\n\t"
    "v_cvt_pk_bf16_f32 v239, v50, v51\n\t"
    "v_smfmac_f32_16x16x64_bf16 v[56:59], a[236:239], v[76:83], v41 abid:1\n\t"
    "ds_read_b128 v[72:75], v3 offset:8448\n\t"
    "v_mov_b64 v[48:49], v[136:137]\n\t"
    "v_smfmac_f32_16x16x64_bf16 v[56:59], a[252:255], v[84:91], v43 abid:1\n\t"
    "s_add_u32 m0, s17, 12288\n\t"
    "ds_read_b128 v[76:79], v4 offset:8448\n\t"
    "v_mov_b64 v[50:51], v[138:139]\n\t"
    "v_smfmac_f32_16x16x64_bf16 v[56:59], v[176:179], v[124:131], v10\n\t"
    "buffer_load_dwordx4 v7, s[8:11], s16 offen offset:0  lds\n\t"
    "s_waitcnt lgkmcnt(6)\n\t"
    "v_cvt_pk_bf16_f32 v240, v52, v53\n\t"
    "v_smfmac_f32_16x16x64_bf16 v[44:47], a[0:3], v[92:99], v12\n\t"
    "ds_read_b128 v[80:83], v5 offset:8448\n\t"
    "v_cvt_pk_bf16_f32 v241, v54, v55\n\t"
    "v_smfmac_f32_16x16x64_bf16 v[48:51], a[4:7], v[92:99], v12 abid:1\n\t"
    "ds_read_b64 v[118:119], v213 offset:8192\n\t"
    "v_mov_b64 v[52:53], v[140:141]\n\t"
    "v_smfmac_f32_16x16x64_bf16 v[44:47], a[16:19], v[60:67], v14\n\t"
    "ds_read_b64 v[120:121], v214 offset:8192\n\t"
    "v_mov_b64 v[54:55], v[142:143]\n\t"
    "v_smfmac_f32_16x16x64_bf16 v[48:51], a[20:23], v[60:67], v14 abid:1\n\t"
    "ds_read_b64 v[122:123], v215 offset:8192\n\t"
    "s_waitcnt lgkmcnt(5)\n\t"
    "v_cvt_pk_bf16_f32 v242, v56, v57\n\t"
    "v_smfmac_f32_16x16x64_bf16 v[44:47], a[32:35], v[68:75], v16\n\t"
    "ds_read_b64 v[124:125], v220 offset:8192\n\t"
    "v_cvt_pk_bf16_f32 v243, v58, v59\n\t"
    "v_smfmac_f32_16x16x64_bf16 v[52:55], a[8:11], v[92:99], v13\n\t"
    "ds_read_b64 v[126:127], v221 offset:8192\n\t"
    "v_mov_b64 v[56:57], v[144:145]\n\t"
    "v_smfmac_f32_16x16x64_bf16 v[48:51], a[36:39], v[68:75], v16 abid:1\n\t"
    "buffer_load_dwordx4 v7, s[8:11], s16 offen offset:1024  lds\n\t"
    "ds_read_b64 v[128:129], v222 offset:8192\n\t"
    "v_mov_b64 v[58:59], v[146:147]\n\t"
    "v_smfmac_f32_16x16x64_bf16 v[52:55], a[24:27], v[60:67], v15\n\t"
    "ds_read_b64 v[130:131], v223 offset:8192\n\t"
    "v_mov_b32_dpp v244, v240 row_ror:8 row_mask:0xf bank_mask:0xf\n\t"
    "v_smfmac_f32_16x16x64_bf16 v[52:55], a[40:43], v[68:75], v17\n\t"
    "ds_read_b128 v[84:87], v2 offset:8704\n\t"
    "v_mov_b32_dpp v245, v241 row_ror:8 row_mask:0xf bank_mask:0xf\n\t"
    "v_smfmac_f32_16x16x64_bf16 v[56:59], a[12:15], v[92:99], v13 abid:1\n\t"
    "ds_read_b128 v[88:91], v3 offset:8704\n\t"
    "v_mov_b32_dpp v246, v242 row_ror:8 row_mask:0xf bank_mask:0xf\n\t"
    "v_smfmac_f32_16x16x64_bf16 v[56:59], a[28:31], v[60:67], v15 abid:1\n\t"
    "ds_read_b128 v[92:95], v4 offset:8704\n\t"
    "v_mov_b32_dpp v247, v243 row_ror:8 row_mask:0xf bank_mask:0xf\n\t"
    "v_smfmac_f32_16x16x64_bf16 v[56:59], a[44:47], v[68:75], v17 abid:1\n\t"
    "ds_read_b128 v[96:99], v5 offset:8704\n\t"
    "s_waitcnt lgkmcnt(7)\n\t"
    "v_mov_b32_dpp v240, v236 row_ror:8 row_mask:0xf bank_mask:0x3\n\t"
    "v_smfmac_f32_16x16x64_bf16 v[44:47], a[48:51], v[76:83], v18\n\t"
    "s_add_u32 m0, s17, 22528\n\t"
    "ds_read_b128 v[60:63], v2 offset:8960\n\t"
    "v_mov_b32_dpp v241, v237 row_ror:8 row_mask:0xf bank_mask:0x3\n\t"
    "v_smfmac_f32_16x16x64_bf16 v[48:51], a[52:55], v[76:83], v18 abid:1\n\t"
    "buffer_load_dwordx4 v8, s[8:11], s16 offen offset:0  lds\n\t"
    "ds_read_b128 v[64:67], v3 offset:8960\n\t"
    "v_mov_b32_dpp v242, v238 row_ror:8 row_mask:0xf bank_mask:0x3\n\t"
    "v_smfmac_f32_16x16x64_bf16 v[52:55], a[56:59], v[76:83], v19\n\t"
    "ds_read_b128 v[68:71], v4 offset:8960\n\t"
    "v_mov_b32_dpp v243, v239 row_ror:8 row_mask:0xf bank_mask:0x3\n\t"
    "v_smfmac_f32_16x16x64_bf16 v[56:59], a[60:63], v[76:83], v19 abid:1\n\t"
    "ds_read_b128 v[72:75], v5 offset:8960\n\t"
    "v_mov_b32_dpp v236, v244 quad_perm:[0,1,2,3] row_mask:0xf bank_mask:0xc\n\t"
    "v_smfmac_f32_16x16x64_bf16 v[44:47], v[148:151], v[100:107], v10\n\t"
    "ds_read_b128 v[76:79], v2 offset:9216\n\t"
    "v_mov_b32_dpp v237, v245 quad_perm:[0,1,2,3] row_mask:0xf bank_mask:0xc\n\t"
    "v_smfmac_f32_16x16x64_bf16 v[48:51], v[152:155], v[108:115], v10\n\t"
    "ds_read_b128 v[80:83], v3 offset:9216\n\t"
    "v_mov_b32_dpp v238, v246 quad_perm:[0,1,2,3] row_mask:0xf bank_mask:0xc\n\t"
    "v_smfmac_f32_16x16x64_bf16 v[52:55], v[156:159], v[116:123], v10\n\t"
    "ds_read_b64 v[100:101], v200 offset:8192\n\t"
    "s_waitcnt lgkmcnt(7)\n\t"
    "v_mov_b32_dpp v239, v247 quad_perm:[0,1,2,3] row_mask:0xf bank_mask:0xc\n\t"
    "v_smfmac_f32_16x16x64_bf16 v[56:59], v[160:163], v[124:131], v10\n\t"
    "ds_read_b64 v[102:103], v201 offset:8192\n\t"
    "v_smfmac_f32_16x16x64_bf16 v[44:47], a[64:67], v[84:91], v20\n\t"
    "buffer_load_dwordx4 v8, s[8:11], s16 offen offset:1024  lds\n\t"
    "ds_read_b64 v[104:105], v202 offset:8192\n\t"
    "v_smfmac_f32_16x16x64_bf16 v[48:51], a[68:71], v[84:91], v20 abid:1\n\t"
    "buffer_store_dwordx4 v[236:239], v0, s[48:51], s55 offen sc1\n\t"
    "ds_read_b64 v[106:107], v203 offset:8192\n\t"
    "v_smfmac_f32_16x16x64_bf16 v[52:55], a[72:75], v[84:91], v21\n\t"
    "ds_read_b64 v[108:109], v208 offset:8192\n\t"
    "v_smfmac_f32_16x16x64_bf16 v[56:59], a[76:79], v[84:91], v21 abid:1\n\t"
    "ds_read_b128 v[84:87], v4 offset:9216\n\t"
    "v_smfmac_f32_16x16x64_bf16 v[44:47], a[80:83], v[92:99], v22\n\t"
    "ds_read_b128 v[88:91], v5 offset:9216\n\t"
    "v_smfmac_f32_16x16x64_bf16 v[48:51], a[84:87], v[92:99], v22 abid:1\n\t"
    "ds_read_b64 v[110:111], v209 offset:8192\n\t"
    "v_smfmac_f32_16x16x64_bf16 v[52:55], a[88:91], v[92:99], v23\n\t"
    "s_add_u32 m0, s17, 32768\n\t"
    "ds_read_b64 v[112:113], v210 offset:8192\n\t"
    "v_smfmac_f32_16x16x64_bf16 v[56:59], a[92:95], v[92:99], v23 abid:1\n\t"
    "buffer_load_dwordx4 v9, s[8:11], s16 offen offset:0  lds\n\t"
    "buffer_store_dwordx4 v[240:243], v1, s[48:51], s55 offen sc1\n\t"
    "s_waitcnt lgkmcnt(6)\n\t"
    "v_smfmac_f32_16x16x64_bf16 v[44:47], a[96:99], v[60:67], v24\n\t"
    "ds_read_b128 v[92:95], v2 offset:9472\n\t"
    "s_add_u32 s54, s54, 32768\n\t"
    "s_xor_b32 s55, s54, s53\n\t"
    "v_smfmac_f32_16x16x64_bf16 v[48:51], a[100:103], v[60:67], v24 abid:1\n\t"
    "ds_read_b128 v[96:99], v3 offset:9472\n\t"
    "v_smfmac_f32_16x16x64_bf16 v[52:55], a[104:107], v[60:67], v25\n\t"
    "ds_read_b64 v[114:115], v211 offset:8192\n\t"
    "v_smfmac_f32_16x16x64_bf16 v[56:59], a[108:111], v[60:67], v25 abid:1\n\t"
    "ds_read_b128 v[60:63], v4 offset:9472\n\t"
    "v_smfmac_f32_16x16x64_bf16 v[44:47], a[112:115], v[68:75], v26\n\t"
    "ds_read_b128 v[64:67], v5 offset:9472\n\t"
    "v_smfmac_f32_16x16x64_bf16 v[48:51], a[116:119], v[68:75], v26 abid:1\n\t"
    "ds_read_b64 v[116:117], v216 offset:8192\n\t"
    "v_smfmac_f32_16x16x64_bf16 v[52:55], a[120:123], v[68:75], v27\n\t"
    "buffer_load_dwordx4 v9, s[8:11], s16 offen offset:1024  lds\n\t"
    "s_add_u32 s52, s52, 32768\n\t"
    "s_xor_b32 s16, s52, s53\n\t"
    "ds_read_b64 v[118:119], v217 offset:8192\n\t"
    "v_smfmac_f32_16x16x64_bf16 v[56:59], a[124:127], v[68:75], v27 abid:1\n\t"
    "ds_read_b128 v[68:71], v2 offset:9728\n\t"
    "v_smfmac_f32_16x16x64_bf16 v[44:47], a[128:131], v[76:83], v28\n\t"
    "ds_read_b128 v[72:75], v3 offset:9728\n\t"
    "v_smfmac_f32_16x16x64_bf16 v[48:51], a[132:135], v[76:83], v28 abid:1\n\t"
    "ds_read_b64 v[120:121], v218 offset:8192\n\t"
    "v_smfmac_f32_16x16x64_bf16 v[52:55], a[136:139], v[76:83], v29\n\t"
    "ds_read_b64 v[122:123], v219 offset:8192\n\t"
    "v_smfmac_f32_16x16x64_bf16 v[56:59], a[140:143], v[76:83], v29 abid:1\n\t"
    "ds_read_b128 v[76:79], v4 offset:9728\n\t"
    "s_waitcnt lgkmcnt(7)\n\t"
    "v_smfmac_f32_16x16x64_bf16 v[44:47], a[144:147], v[84:91], v30\n\t"
    "ds_read_b128 v[80:83], v5 offset:9728\n\t"
    "v_smfmac_f32_16x16x64_bf16 v[48:51], a[148:151], v[84:91], v30 abid:1\n\t"
    "ds_read_b64 v[124:125], v224 offset:8192\n\t"
    "v_smfmac_f32_16x16x64_bf16 v[52:55], a[152:155], v[84:91], v31\n\t"
    "ds_read_b64 v[126:127], v225 offset:8192\n\t"
    "v_smfmac_f32_16x16x64_bf16 v[56:59], a[156:159], v[84:91], v31 abid:1\n\t"
    "ds_read_b128 v[84:87], v2 offset:9984\n\t"
    "v_smfmac_f32_16x16x64_bf16 v[44:47], a[160:163], v[92:99], v32\n\t"
    "ds_read_b128 v[88:91], v3 offset:9984\n\t"
    "v_smfmac_f32_16x16x64_bf16 v[48:51], a[164:167], v[92:99], v32 abid:1\n\t"
    "ds_read_b64 v[128:129], v226 offset:8192\n\t"
    "v_smfmac_f32_16x16x64_bf16 v[52:55], a[168:171], v[92:99], v33\n\t"
    "ds_read_b64 v[130:131], v227 offset:8192\n\t"
    "v_smfmac_f32_16x16x64_bf16 v[56:59], a[172:175], v[92:99], v33 abid:1\n\t"
    "ds_read_b128 v[92:95], v4 offset:9984\n\t"
    "v_smfmac_f32_16x16x64_bf16 v[44:47], a[176:179], v[60:67], v34\n\t"
    "ds_read_b128 v[96:99], v5 offset:9984\n\t"
    "v_smfmac_f32_16x16x64_bf16 v[48:51], a[180:183], v[60:67], v34 abid:1\n\t"
    "v_smfmac_f32_16x16x64_bf16 v[52:55], a[184:187], v[60:67], v35\n\t"
    "v_smfmac_f32_16x16x64_bf16 v[56:59], a[188:191], v[60:67], v35 abid:1\n\t"
    "s_waitcnt lgkmcnt(4)\n\t"
    "v_smfmac_f32_16x16x64_bf16 v[44:47], a[192:195], v[68:75], v36\n\t"
    "v_smfmac_f32_16x16x64_bf16 v[48:51], a[196:199], v[68:75], v36 abid:1\n\t"
    "v_smfmac_f32_16x16x64_bf16 v[52:55], a[200:203], v[68:75], v37\n\t"
    "v_smfmac_f32_16x16x64_bf16 v[56:59], a[204:207], v[68:75], v37 abid:1\n\t"
    "s_waitcnt vmcnt(10) lgkmcnt(0)\n\t"
    "s_barrier\n\t"
    "ds_read_b128 v[60:63], v2 offset:0\n\t"
    "v_smfmac_f32_16x16x64_bf16 v[44:47], a[208:211], v[76:83], v38\n\t"
    "s_add_u32 m0, s17, 4096\n\t"
    "ds_read_b128 v[64:67], v3 offset:0\n\t"
    "v_smfmac_f32_16x16x64_bf16 v[44:47], a[224:227], v[84:91], v40\n\t"
    "buffer_load_dwordx4 v6, s[8:11], s16 offen offset:0  lds\n\t"
    "ds_read_b128 v[68:71], v4 offset:0\n\t"
    "v_smfmac_f32_16x16x64_bf16 v[44:47], a[240:243], v[92:99], v42\n\t"
    "ds_read_b128 v[72:75], v5 offset:0\n\t"
    "v_smfmac_f32_16x16x64_bf16 v[44:47], v[164:167], v[100:107], v10\n\t"
    "ds_read_b64 v[100:101], v196 offset:0\n\t"
    "v_smfmac_f32_16x16x64_bf16 v[48:51], a[212:215], v[76:83], v38 abid:1\n\t"
    "ds_read_b64 v[102:103], v197 offset:0\n\t"
    "v_smfmac_f32_16x16x64_bf16 v[48:51], a[228:231], v[84:91], v40 abid:1\n\t"
    "ds_read_b64 v[104:105], v198 offset:0\n\t"
    "v_smfmac_f32_16x16x64_bf16 v[48:51], a[244:247], v[92:99], v42 abid:1\n\t"
    "ds_read_b64 v[106:107], v199 offset:0\n\t"
    "v_smfmac_f32_16x16x64_bf16 v[48:51], v[168:171], v[108:115], v10\n\t"
    "ds_read_b64 v[108:109], v204 offset:0\n\t"
    "v_cvt_pk_bf16_f32 v236, v44, v45\n\t"
    "v_smfmac_f32_16x16x64_bf16 v[52:55], a[216:219], v[76:83], v39\n\t"
    "buffer_load_dwordx4 v6, s[8:11], s16 offen offset:1024  lds\n\t"
    "ds_read_b64 v[110:111], v205 offset:0\n\t"
    "v_cvt_pk_bf16_f32 v237, v46, v47\n\t"
    "v_smfmac_f32_16x16x64_bf16 v[52:55], a[232:235], v[84:91], v41\n\t"
    "ds_read_b64 v[112:113], v206 offset:0\n\t"
    "v_mov_b64 v[44:45], v[132:133]\n\t"
    "v_smfmac_f32_16x16x64_bf16 v[52:55], a[248:251], v[92:99], v43\n\t"
    "ds_read_b64 v[114:115], v207 offset:0\n\t"
    "v_mov_b64 v[46:47], v[134:135]\n\t"
    "v_smfmac_f32_16x16x64_bf16 v[52:55], v[172:175], v[116:123], v10\n\t"
    "ds_read_b64 v[116:117], v212 offset:0\n\t"
    "v_cvt_pk_bf16_f32 v238, v48, v49\n\t"
    "v_smfmac_f32_16x16x64_bf16 v[56:59], a[220:223], v[76:83], v39 abid:1\n\t"
    "ds_read_b128 v[76:79], v2 offset:256\n\t"
    "v_cvt_pk_bf16_f32 v239, v50, v51\n\t"
    "v_smfmac_f32_16x16x64_bf16 v[56:59], a[236:239], v[84:91], v41 abid:1\n\t"
    "ds_read_b128 v[80:83], v3 offset:256\n\t"
    "v_mov_b64 v[48:49], v[136:137]\n\t"
    "v_smfmac_f32_16x16x64_bf16 v[56:59], a[252:255], v[92:99], v43 abid:1\n\t"
    "s_add_u32 m0, s17, 14336\n\t"
    "ds_read_b128 v[84:87], v4 offset:256\n\t"
    "v_mov_b64 v[50:51], v[138:139]\n\t"
    "v_smfmac_f32_16x16x64_bf16 v[56:59], v[176:179], v[124:131], v10\n\t"
    "buffer_load_dwordx4 v7, s[8:11], s16 offen offset:0  lds\n\t"
    "s_waitcnt lgkmcnt(6)\n\t"
    "v_cvt_pk_bf16_f32 v240, v52, v53\n\t"
    "v_smfmac_f32_16x16x64_bf16 v[44:47], a[0:3], v[60:67], v12\n\t"
    "ds_read_b128 v[88:91], v5 offset:256\n\t"
    "v_cvt_pk_bf16_f32 v241, v54, v55\n\t"
    "v_smfmac_f32_16x16x64_bf16 v[48:51], a[4:7], v[60:67], v12 abid:1\n\t"
    "ds_read_b64 v[118:119], v213 offset:0\n\t"
    "v_mov_b64 v[52:53], v[140:141]\n\t"
    "v_smfmac_f32_16x16x64_bf16 v[44:47], a[16:19], v[68:75], v14\n\t"
    "ds_read_b64 v[120:121], v214 offset:0\n\t"
    "v_mov_b64 v[54:55], v[142:143]\n\t"
    "v_smfmac_f32_16x16x64_bf16 v[48:51], a[20:23], v[68:75], v14 abid:1\n\t"
    "ds_read_b64 v[122:123], v215 offset:0\n\t"
    "s_waitcnt lgkmcnt(5)\n\t"
    "v_cvt_pk_bf16_f32 v242, v56, v57\n\t"
    "v_smfmac_f32_16x16x64_bf16 v[44:47], a[32:35], v[76:83], v16\n\t"
    "ds_read_b64 v[124:125], v220 offset:0\n\t"
    "v_cvt_pk_bf16_f32 v243, v58, v59\n\t"
    "v_smfmac_f32_16x16x64_bf16 v[52:55], a[8:11], v[60:67], v13\n\t"
    "ds_read_b64 v[126:127], v221 offset:0\n\t"
    "v_mov_b64 v[56:57], v[144:145]\n\t"
    "v_smfmac_f32_16x16x64_bf16 v[48:51], a[36:39], v[76:83], v16 abid:1\n\t"
    "buffer_load_dwordx4 v7, s[8:11], s16 offen offset:1024  lds\n\t"
    "ds_read_b64 v[128:129], v222 offset:0\n\t"
    "v_mov_b64 v[58:59], v[146:147]\n\t"
    "v_smfmac_f32_16x16x64_bf16 v[52:55], a[24:27], v[68:75], v15\n\t"
    "ds_read_b64 v[130:131], v223 offset:0\n\t"
    "v_mov_b32_dpp v244, v240 row_ror:8 row_mask:0xf bank_mask:0xf\n\t"
    "v_smfmac_f32_16x16x64_bf16 v[52:55], a[40:43], v[76:83], v17\n\t"
    "ds_read_b128 v[92:95], v2 offset:512\n\t"
    "v_mov_b32_dpp v245, v241 row_ror:8 row_mask:0xf bank_mask:0xf\n\t"
    "v_smfmac_f32_16x16x64_bf16 v[56:59], a[12:15], v[60:67], v13 abid:1\n\t"
    "ds_read_b128 v[96:99], v3 offset:512\n\t"
    "v_mov_b32_dpp v246, v242 row_ror:8 row_mask:0xf bank_mask:0xf\n\t"
    "v_smfmac_f32_16x16x64_bf16 v[56:59], a[28:31], v[68:75], v15 abid:1\n\t"
    "ds_read_b128 v[60:63], v4 offset:512\n\t"
    "v_mov_b32_dpp v247, v243 row_ror:8 row_mask:0xf bank_mask:0xf\n\t"
    "v_smfmac_f32_16x16x64_bf16 v[56:59], a[44:47], v[76:83], v17 abid:1\n\t"
    "ds_read_b128 v[64:67], v5 offset:512\n\t"
    "s_waitcnt lgkmcnt(7)\n\t"
    "v_mov_b32_dpp v240, v236 row_ror:8 row_mask:0xf bank_mask:0x3\n\t"
    "v_smfmac_f32_16x16x64_bf16 v[44:47], a[48:51], v[84:91], v18\n\t"
    "s_add_u32 m0, s17, 24576\n\t"
    "ds_read_b128 v[68:71], v2 offset:768\n\t"
    "v_mov_b32_dpp v241, v237 row_ror:8 row_mask:0xf bank_mask:0x3\n\t"
    "v_smfmac_f32_16x16x64_bf16 v[48:51], a[52:55], v[84:91], v18 abid:1\n\t"
    "buffer_load_dwordx4 v8, s[8:11], s16 offen offset:0  lds\n\t"
    "ds_read_b128 v[72:75], v3 offset:768\n\t"
    "v_mov_b32_dpp v242, v238 row_ror:8 row_mask:0xf bank_mask:0x3\n\t"
    "v_smfmac_f32_16x16x64_bf16 v[52:55], a[56:59], v[84:91], v19\n\t"
    "ds_read_b128 v[76:79], v4 offset:768\n\t"
    "v_mov_b32_dpp v243, v239 row_ror:8 row_mask:0xf bank_mask:0x3\n\t"
    "v_smfmac_f32_16x16x64_bf16 v[56:59], a[60:63], v[84:91], v19 abid:1\n\t"
    "ds_read_b128 v[80:83], v5 offset:768\n\t"
    "v_mov_b32_dpp v236, v244 quad_perm:[0,1,2,3] row_mask:0xf bank_mask:0xc\n\t"
    "v_smfmac_f32_16x16x64_bf16 v[44:47], v[148:151], v[100:107], v10\n\t"
    "ds_read_b128 v[84:87], v2 offset:1024\n\t"
    "v_mov_b32_dpp v237, v245 quad_perm:[0,1,2,3] row_mask:0xf bank_mask:0xc\n\t"
    "v_smfmac_f32_16x16x64_bf16 v[48:51], v[152:155], v[108:115], v10\n\t"
    "ds_read_b128 v[88:91], v3 offset:1024\n\t"
    "v_mov_b32_dpp v238, v246 quad_perm:[0,1,2,3] row_mask:0xf bank_mask:0xc\n\t"
    "v_smfmac_f32_16x16x64_bf16 v[52:55], v[156:159], v[116:123], v10\n\t"
    "ds_read_b64 v[100:101], v200 offset:0\n\t"
    "s_waitcnt lgkmcnt(7)\n\t"
    "v_mov_b32_dpp v239, v247 quad_perm:[0,1,2,3] row_mask:0xf bank_mask:0xc\n\t"
    "v_smfmac_f32_16x16x64_bf16 v[56:59], v[160:163], v[124:131], v10\n\t"
    "ds_read_b64 v[102:103], v201 offset:0\n\t"
    "v_smfmac_f32_16x16x64_bf16 v[44:47], a[64:67], v[92:99], v20\n\t"
    "buffer_load_dwordx4 v8, s[8:11], s16 offen offset:1024  lds\n\t"
    "ds_read_b64 v[104:105], v202 offset:0\n\t"
    "v_smfmac_f32_16x16x64_bf16 v[48:51], a[68:71], v[92:99], v20 abid:1\n\t"
    "buffer_store_dwordx4 v[236:239], v0, s[48:51], s55 offen sc1\n\t"
    "ds_read_b64 v[106:107], v203 offset:0\n\t"
    "v_smfmac_f32_16x16x64_bf16 v[52:55], a[72:75], v[92:99], v21\n\t"
    "ds_read_b64 v[108:109], v208 offset:0\n\t"
    "v_smfmac_f32_16x16x64_bf16 v[56:59], a[76:79], v[92:99], v21 abid:1\n\t"
    "ds_read_b128 v[92:95], v4 offset:1024\n\t"
    "v_smfmac_f32_16x16x64_bf16 v[44:47], a[80:83], v[60:67], v22\n\t"
    "ds_read_b128 v[96:99], v5 offset:1024\n\t"
    "v_smfmac_f32_16x16x64_bf16 v[48:51], a[84:87], v[60:67], v22 abid:1\n\t"
    "ds_read_b64 v[110:111], v209 offset:0\n\t"
    "v_smfmac_f32_16x16x64_bf16 v[52:55], a[88:91], v[60:67], v23\n\t"
    "s_add_u32 m0, s17, 34816\n\t"
    "ds_read_b64 v[112:113], v210 offset:0\n\t"
    "v_smfmac_f32_16x16x64_bf16 v[56:59], a[92:95], v[60:67], v23 abid:1\n\t"
    "buffer_load_dwordx4 v9, s[8:11], s16 offen offset:0  lds\n\t"
    "buffer_store_dwordx4 v[240:243], v1, s[48:51], s55 offen sc1\n\t"
    "s_waitcnt lgkmcnt(6)\n\t"
    "v_smfmac_f32_16x16x64_bf16 v[44:47], a[96:99], v[68:75], v24\n\t"
    "ds_read_b128 v[60:63], v2 offset:1280\n\t"
    "s_add_u32 s54, s54, 32768\n\t"
    "s_xor_b32 s55, s54, s53\n\t"
    "v_smfmac_f32_16x16x64_bf16 v[48:51], a[100:103], v[68:75], v24 abid:1\n\t"
    "ds_read_b128 v[64:67], v3 offset:1280\n\t"
    "v_smfmac_f32_16x16x64_bf16 v[52:55], a[104:107], v[68:75], v25\n\t"
    "ds_read_b64 v[114:115], v211 offset:0\n\t"
    "v_smfmac_f32_16x16x64_bf16 v[56:59], a[108:111], v[68:75], v25 abid:1\n\t"
    "ds_read_b128 v[68:71], v4 offset:1280\n\t"
    "v_smfmac_f32_16x16x64_bf16 v[44:47], a[112:115], v[76:83], v26\n\t"
    "ds_read_b128 v[72:75], v5 offset:1280\n\t"
    "v_smfmac_f32_16x16x64_bf16 v[48:51], a[116:119], v[76:83], v26 abid:1\n\t"
    "ds_read_b64 v[116:117], v216 offset:0\n\t"
    "v_smfmac_f32_16x16x64_bf16 v[52:55], a[120:123], v[76:83], v27\n\t"
    "buffer_load_dwordx4 v9, s[8:11], s16 offen offset:1024  lds\n\t"
    "s_add_u32 s52, s52, 32768\n\t"
    "s_xor_b32 s16, s52, s53\n\t"
    "ds_read_b64 v[118:119], v217 offset:0\n\t"
    "v_smfmac_f32_16x16x64_bf16 v[56:59], a[124:127], v[76:83], v27 abid:1\n\t"
    "ds_read_b128 v[76:79], v2 offset:1536\n\t"
    "v_smfmac_f32_16x16x64_bf16 v[44:47], a[128:131], v[84:91], v28\n\t"
    "ds_read_b128 v[80:83], v3 offset:1536\n\t"
    "v_smfmac_f32_16x16x64_bf16 v[48:51], a[132:135], v[84:91], v28 abid:1\n\t"
    "ds_read_b64 v[120:121], v218 offset:0\n\t"
    "v_smfmac_f32_16x16x64_bf16 v[52:55], a[136:139], v[84:91], v29\n\t"
    "ds_read_b64 v[122:123], v219 offset:0\n\t"
    "v_smfmac_f32_16x16x64_bf16 v[56:59], a[140:143], v[84:91], v29 abid:1\n\t"
    "ds_read_b128 v[84:87], v4 offset:1536\n\t"
    "s_waitcnt lgkmcnt(7)\n\t"
    "v_smfmac_f32_16x16x64_bf16 v[44:47], a[144:147], v[92:99], v30\n\t"
    "ds_read_b128 v[88:91], v5 offset:1536\n\t"
    "v_smfmac_f32_16x16x64_bf16 v[48:51], a[148:151], v[92:99], v30 abid:1\n\t"
    "ds_read_b64 v[124:125], v224 offset:0\n\t"
    "v_smfmac_f32_16x16x64_bf16 v[52:55], a[152:155], v[92:99], v31\n\t"
    "ds_read_b64 v[126:127], v225 offset:0\n\t"
    "v_smfmac_f32_16x16x64_bf16 v[56:59], a[156:159], v[92:99], v31 abid:1\n\t"
    "ds_read_b128 v[92:95], v2 offset:1792\n\t"
    "v_smfmac_f32_16x16x64_bf16 v[44:47], a[160:163], v[60:67], v32\n\t"
    "ds_read_b128 v[96:99], v3 offset:1792\n\t"
    "v_smfmac_f32_16x16x64_bf16 v[48:51], a[164:167], v[60:67], v32 abid:1\n\t"
    "ds_read_b64 v[128:129], v226 offset:0\n\t"
    "v_smfmac_f32_16x16x64_bf16 v[52:55], a[168:171], v[60:67], v33\n\t"
    "ds_read_b64 v[130:131], v227 offset:0\n\t"
    "v_smfmac_f32_16x16x64_bf16 v[56:59], a[172:175], v[60:67], v33 abid:1\n\t"
    "ds_read_b128 v[60:63], v4 offset:1792\n\t"
    "v_smfmac_f32_16x16x64_bf16 v[44:47], a[176:179], v[68:75], v34\n\t"
    "ds_read_b128 v[64:67], v5 offset:1792\n\t"
    "v_smfmac_f32_16x16x64_bf16 v[48:51], a[180:183], v[68:75], v34 abid:1\n\t"
    "v_smfmac_f32_16x16x64_bf16 v[52:55], a[184:187], v[68:75], v35\n\t"
    "v_smfmac_f32_16x16x64_bf16 v[56:59], a[188:191], v[68:75], v35 abid:1\n\t"
    "s_waitcnt lgkmcnt(4)\n\t"
    "v_smfmac_f32_16x16x64_bf16 v[44:47], a[192:195], v[76:83], v36\n\t"
    "v_smfmac_f32_16x16x64_bf16 v[48:51], a[196:199], v[76:83], v36 abid:1\n\t"
    "v_smfmac_f32_16x16x64_bf16 v[52:55], a[200:203], v[76:83], v37\n\t"
    "v_smfmac_f32_16x16x64_bf16 v[56:59], a[204:207], v[76:83], v37 abid:1\n\t"
    "s_sub_u32 s18, s18, 1\n\t"
    "s_cmp_lg_u32 s18, 0\n\t"
    "s_waitcnt vmcnt(10) lgkmcnt(0)\n\t"
    "s_barrier\n\t"
    "s_cbranch_scc1 .LloopA_%=\n\t"
    "s_or_b32 s26, %3, %4\n\t"
    "s_cmp_lg_u32 s26, 0\n\t"
    "s_cbranch_scc1 .LnfA_%=\n\t"
    "s_mov_b64 s[44:45], %2\n\t"
    "s_add_u32 s44, s44, 0x1000000\n\t"
    "s_addc_u32 s45, s45, 0\n\t"
    "global_load_dwordx4 v[68:71], v11, s[44:45] offset:0 sc1\n\t"
    "global_load_dwordx4 v[72:75], v11, s[44:45] offset:1024 sc1\n\t"
    "global_load_dwordx4 v[76:79], v11, s[44:45] offset:2048 sc1\n\t"
    "global_load_dwordx4 v[80:83], v11, s[44:45] offset:3072 sc1\n\t"
    ".LnfA_%=:\n\t"
    "v_smfmac_f32_16x16x64_bf16 v[44:47], a[208:211], v[84:91], v38\n\t"
    "v_smfmac_f32_16x16x64_bf16 v[44:47], a[224:227], v[92:99], v40\n\t"
    "v_smfmac_f32_16x16x64_bf16 v[44:47], a[240:243], v[60:67], v42\n\t"
    "v_smfmac_f32_16x16x64_bf16 v[44:47], v[164:167], v[100:107], v10\n\t"
    "v_smfmac_f32_16x16x64_bf16 v[48:51], a[212:215], v[84:91], v38 abid:1\n\t"
    "v_smfmac_f32_16x16x64_bf16 v[48:51], a[228:231], v[92:99], v40 abid:1\n\t"
    "v_smfmac_f32_16x16x64_bf16 v[48:51], a[244:247], v[60:67], v42 abid:1\n\t"
    "v_smfmac_f32_16x16x64_bf16 v[48:51], v[168:171], v[108:115], v10\n\t"
    "s_nop 3\n\t"
    "v_cvt_pk_bf16_f32 v236, v44, v45\n\t"
    "v_smfmac_f32_16x16x64_bf16 v[52:55], a[216:219], v[84:91], v39\n\t"
    "s_nop 0\n\t"
    "v_cvt_pk_bf16_f32 v237, v46, v47\n\t"
    "v_smfmac_f32_16x16x64_bf16 v[52:55], a[232:235], v[92:99], v41\n\t"
    "v_smfmac_f32_16x16x64_bf16 v[52:55], a[248:251], v[60:67], v43\n\t"
    "v_smfmac_f32_16x16x64_bf16 v[52:55], v[172:175], v[116:123], v10\n\t"
    "v_cvt_pk_bf16_f32 v238, v48, v49\n\t"
    "v_smfmac_f32_16x16x64_bf16 v[56:59], a[220:223], v[84:91], v39 abid:1\n\t"
    "v_cvt_pk_bf16_f32 v239, v50, v51\n\t"
    "v_smfmac_f32_16x16x64_bf16 v[56:59], a[236:239], v[92:99], v41 abid:1\n\t"
    "v_smfmac_f32_16x16x64_bf16 v[56:59], a[252:255], v[60:67], v43 abid:1\n\t"
    "v_smfmac_f32_16x16x64_bf16 v[56:59], v[176:179], v[124:131], v10\n\t"
    "s_nop 1\n\t"
    "v_cvt_pk_bf16_f32 v240, v52, v53\n\t"
    "v_cvt_pk_bf16_f32 v241, v54, v55\n\t"
    "s_nop 4\n\t"
    "v_cvt_pk_bf16_f32 v242, v56, v57\n\t"
    "s_nop 2\n\t"
    "v_cvt_pk_bf16_f32 v243, v58, v59\n\t"
    "s_nop 1\n\t"
    "v_mov_b32_dpp v244, v240 row_ror:8 row_mask:0xf bank_mask:0xf\n\t"
    "v_mov_b32_dpp v245, v241 row_ror:8 row_mask:0xf bank_mask:0xf\n\t"
    "v_mov_b32_dpp v246, v242 row_ror:8 row_mask:0xf bank_mask:0xf\n\t"
    "v_mov_b32_dpp v247, v243 row_ror:8 row_mask:0xf bank_mask:0xf\n\t"
    "v_mov_b32_dpp v240, v236 row_ror:8 row_mask:0xf bank_mask:0x3\n\t"
    "v_mov_b32_dpp v241, v237 row_ror:8 row_mask:0xf bank_mask:0x3\n\t"
    "v_mov_b32_dpp v242, v238 row_ror:8 row_mask:0xf bank_mask:0x3\n\t"
    "v_mov_b32_dpp v243, v239 row_ror:8 row_mask:0xf bank_mask:0x3\n\t"
    "v_mov_b32_dpp v236, v244 quad_perm:[0,1,2,3] row_mask:0xf bank_mask:0xc\n\t"
    "v_mov_b32_dpp v237, v245 quad_perm:[0,1,2,3] row_mask:0xf bank_mask:0xc\n\t"
    "v_mov_b32_dpp v238, v246 quad_perm:[0,1,2,3] row_mask:0xf bank_mask:0xc\n\t"
    "v_mov_b32_dpp v239, v247 quad_perm:[0,1,2,3] row_mask:0xf bank_mask:0xc\n\t"
    "buffer_store_dwordx4 v[236:239], v0, s[48:51], s55 offen sc1\n\t"
    "buffer_store_dwordx4 v[240:243], v1, s[48:51], s55 offen sc1\n\t"
    "s_add_u32 s54, s54, 32768\n\t"
    "s_xor_b32 s55, s54, s53\n\t"
    "s_mov_b32 s47, 1\n\t"
    "s_branch .Lend_%=\n\t"
    ".LvarB_%=:\n\t"
    "ds_read_b128 v[92:95], v2 offset:0\n\t"
    "ds_read_b128 v[96:99], v3 offset:0\n\t"
    "ds_read_b128 v[60:63], v2 offset:2048\n\t"
    "s_waitcnt lgkmcnt(1)\n\t"
    "s_waitcnt vmcnt(27)\n\t"
    "v_lshrrev_b32 v196, 16, v196\n\t"
    "v_smfmac_f32_16x16x64_bf16 v[100:103], a[0:3], v[92:99], v12\n\t"
    "ds_read_b128 v[64:67], v3 offset:2048\n\t"
    "ds_read_b128 v[68:71], v2 offset:4096\n\t"
    "s_waitcnt vmcnt(26)\n\t"
    "v_xad_u32 v196, v196, v253, v252\n\t"
    "v_smfmac_f32_16x16x64_bf16 v[104:107], a[4:7], v[92:99], v12 abid:1\n\t"
    "ds_read_b128 v[72:75], v3 offset:4096\n\t"
    "ds_read_b128 v[76:79], v2 offset:6144\n\t"
    "s_add_u32 m0, s17, 6144\n\t"
    "s_add_u32 s40, s53, 98304\n\t"
    "buffer_load_dwordx4 v6, s[8:11], s40 offen offset:1024  lds\n\t"
    "s_waitcnt vmcnt(26)\n\t"
    "v_lshrrev_b32 v197, 16, v197\n\t"
    "v_smfmac_f32_16x16x64_bf16 v[108:111], a[8:11], v[92:99], v13\n\t"
    "ds_read_b128 v[80:83], v3 offset:6144\n\t"
    "s_waitcnt vmcnt(25)\n\t"
    "v_xad_u32 v197, v197, v253, v252\n\t"
    "v_smfmac_f32_16x16x64_bf16 v[112:115], a[12:15], v[92:99], v13 abid:1\n\t"
    "s_add_u32 m0, s17, 16384\n\t"
    "s_add_u32 s40, s53, 98304\n\t"
    "buffer_load_dwordx4 v7, s[8:11], s40 offen offset:1024  lds\n\t"
    "s_waitcnt lgkmcnt(4)\n\t"
    "v_lshrrev_b32 v198, 16, v198\n\t"
    "v_smfmac_f32_16x16x64_bf16 v[116:119], a[0:3], v[60:67], v12\n\t"
    "ds_read_b128 v[84:87], v4 offset:0\n\t"
    "v_xad_u32 v198, v198, v253, v252\n\t"
    "v_smfmac_f32_16x16x64_bf16 v[120:123], a[4:7], v[60:67], v12 abid:1\n\t"
    "ds_read_b128 v[88:91], v5 offset:0\n\t"
    "s_add_u32 m0, s17, 26624\n\t"
    "s_add_u32 s40, s53, 98304\n\t"
    "buffer_load_dwordx4 v8, s[8:11], s40 offen offset:1024  lds\n\t"
    "v_lshrrev_b32 v199, 16, v199\n\t"
    "v_smfmac_f32_16x16x64_bf16 v[124:127], a[8:11], v[60:67], v13\n\t"
    "ds_read_b128 v[92:95], v4 offset:2048\n\t"
    "v_xad_u32 v199, v199, v253, v252\n\t"
    "v_smfmac_f32_16x16x64_bf16 v[128:131], a[12:15], v[60:67], v13 abid:1\n\t"
    "ds_read_b128 v[96:99], v5 offset:2048\n\t"
    "s_waitcnt lgkmcnt(6)\n\t"
    "v_lshrrev_b32 v200, 16, v200\n\t"
    "v_smfmac_f32_16x16x64_bf16 v[236:239], a[0:3], v[68:75], v12\n\t"
    "s_add_u32 m0, s17, 36864\n\t"
    "s_add_u32 s40, s53, 98304\n\t"
    "buffer_load_dwordx4 v9, s[8:11], s40 offen offset:1024  lds\n\t"
    "ds_read_b128 v[60:63], v4 offset:4096\n\t"
    "v_xad_u32 v200, v200, v253, v252\n\t"
    "v_smfmac_f32_16x16x64_bf16 v[240:243], a[4:7], v[68:75], v12 abid:1\n\t"
    "ds_read_b128 v[64:67], v5 offset:4096\n\t"
    "v_lshrrev_b32 v201, 16, v201\n\t"
    "v_smfmac_f32_16x16x64_bf16 v[244:247], a[8:11], v[68:75], v13\n\t"
    "v_xad_u32 v201, v201, v253, v252\n\t"
    "v_smfmac_f32_16x16x64_bf16 v[248:251], a[12:15], v[68:75], v13 abid:1\n\t"
    "global_load_dwordx4 a[64:67], v11, s[30:31] offset:0 sc1\n\t"
    "s_waitcnt lgkmcnt(5)\n\t"
    "v_lshrrev_b32 v202, 16, v202\n\t"
    "v_smfmac_f32_16x16x64_bf16 v[44:47], a[0:3], v[76:83], v12\n\t"
    "ds_read_b128 v[68:71], v4 offset:6144\n\t"
    "v_xad_u32 v202, v202, v253, v252\n\t"
    "v_smfmac_f32_16x16x64_bf16 v[48:51], a[4:7], v[76:83], v12 abid:1\n\t"
    "ds_read_b128 v[72:75], v5 offset:6144\n\t"
    "v_lshrrev_b32 v203, 16, v203\n\t"
    "v_smfmac_f32_16x16x64_bf16 v[52:55], a[8:11], v[76:83], v13\n\t"
    "global_load_dwordx4 a[68:71], v11, s[30:31] offset:1024 sc1\n\t"
    "v_xad_u32 v203, v203, v253, v252\n\t"
    "v_smfmac_f32_16x16x64_bf16 v[56:59], a[12:15], v[76:83], v13 abid:1\n\t"
    "s_waitcnt lgkmcnt(3)\n\t"
    "s_waitcnt vmcnt(29)\n\t"
    "v_lshrrev_b32 v204, 16, v204\n\t"
    "v_smfmac_f32_16x16x64_bf16 v[100:103], a[16:19], v[84:91], v14\n\t"
    "ds_read_b128 v[76:79], v2 offset:256\n\t"
    "s_waitcnt vmcnt(28)\n\t"
    "v_xad_u32 v204, v204, v253, v252\n\t"
    "v_smfmac_f32_16x16x64_bf16 v[104:107], a[20:23], v[84:91], v14 abid:1\n\t"
    "global_load_dwordx4 a[72:75], v11, s[30:31] offset:2048 sc1\n\t"
    "s_waitcnt vmcnt(28)\n\t"
    "v_lshrrev_b32 v205, 16, v205\n\t"
    "v_smfmac_f32_16x16x64_bf16 v[108:111], a[24:27], v[84:91], v15\n\t"
    "ds_read_b128 v[80:83], v3 offset:256\n\t"
    "s_waitcnt vmcnt(27)\n\t"
    "v_xad_u32 v205, v205, v253, v252\n\t"
    "v_smfmac_f32_16x16x64_bf16 v[112:115], a[28:31], v[84:91], v15 abid:1\n\t"
    "ds_read_b128 v[84:87], v2 offset:2304\n\t"
    "v_lshrrev_b32 v206, 16, v206\n\t"
    "v_smfmac_f32_16x16x64_bf16 v[116:119], a[16:19], v[92:99], v14\n\t"
    "global_load_dwordx4 a[76:79], v11, s[30:31] offset:3072 sc1\n\t"
    "s_add_u32 s30, s30, 4096\n\t"
    "s_addc_u32 s31, s31, 0\n\t"
    "ds_read_b128 v[88:91], v3 offset:2304\n\t"
    "v_xad_u32 v206, v206, v253, v252\n\t"
    "v_smfmac_f32_16x16x64_bf16 v[120:123], a[20:23], v[92:99], v14 abid:1\n\t"
    "v_lshrrev_b32 v207, 16, v207\n\t"
    "v_smfmac_f32_16x16x64_bf16 v[124:127], a[24:27], v[92:99], v15\n\t"
    "v_xad_u32 v207, v207, v253, v252\n\t"
    "v_smfmac_f32_16x16x64_bf16 v[128:131], a[28:31], v[92:99], v15 abid:1\n\t"
    "global_load_dwordx4 a[80:83], v11, s[30:31] offset:0 sc1\n\t"
    "s_waitcnt lgkmcnt(3)\n\t"
    "v_lshrrev_b32 v208, 16, v208\n\t"
    "v_smfmac_f32_16x16x64_bf16 v[236:239], a[16:19], v[60:67], v14\n\t"
    "ds_read_b128 v[92:95], v2 offset:4352\n\t"
    "v_xad_u32 v208, v208, v253, v252\n\t"
    "v_smfmac_f32_16x16x64_bf16 v[240:243], a[20:23], v[60:67], v14 abid:1\n\t"
    "ds_read_b128 v[96:99], v3 offset:4352\n\t"
    "v_lshrrev_b32 v209, 16, v209\n\t"
    "v_smfmac_f32_16x16x64_bf16 v[244:247], a[24:27], v[60:67], v15\n\t"
    "global_load_dwordx4 a[84:87], v11, s[30:31] offset:1024 sc1\n\t"
    "v_xad_u32 v209, v209, v253, v252\n\t"
    "v_smfmac_f32_16x16x64_bf16 v[248:251], a[28:31], v[60:67], v15 abid:1\n\t"
    "ds_read_b128 v[60:63], v2 offset:6400\n\t"
    "v_lshrrev_b32 v210, 16, v210\n\t"
    "v_smfmac_f32_16x16x64_bf16 v[44:47], a[16:19], v[68:75], v14\n\t"
    "ds_read_b128 v[64:67], v3 offset:6400\n\t"
    "v_xad_u32 v210, v210, v253, v252\n\t"
    "v_smfmac_f32_16x16x64_bf16 v[48:51], a[20:23], v[68:75], v14 abid:1\n\t"
    "global_load_dwordx4 a[88:91], v11, s[30:31] offset:2048 sc1\n\t"
    "v_lshrrev_b32 v211, 16, v211\n\t"
    "v_smfmac_f32_16x16x64_bf16 v[52:55], a[24:27], v[68:75], v15\n\t"
    "v_xad_u32 v211, v211, v253, v252\n\t"
    "v_smfmac_f32_16x16x64_bf16 v[56:59], a[28:31], v[68:75], v15 abid:1\n\t"
    "s_waitcnt lgkmcnt(3)\n\t"
    "s_waitcnt vmcnt(30)\n\t"
    "v_lshrrev_b32 v212, 16, v212\n\t"
    "v_smfmac_f32_16x16x64_bf16 v[100:103], a[32:35], v[76:83], v16\n\t"
    "global_load_dwordx4 a[92:95], v11, s[30:31] offset:3072 sc1\n\t"
    "s_add_u32 s30, s30, 4096\n\t"
    "s_addc_u32 s31, s31, 0\n\t"
    "s_waitcnt vmcnt(30)\n\t"
    "v_xad_u32 v212, v212, v253, v252\n\t"
    "v_smfmac_f32_16x16x64_bf16 v[104:107], a[36:39], v[76:83], v16 abid:1\n\t"
    "ds_read_b128 v[68:71], v4 offset:256\n\t"
    "s_waitcnt vmcnt(29)\n\t"
    "v_lshrrev_b32 v213, 16, v213\n\t"
    "v_smfmac_f32_16x16x64_bf16 v[108:111], a[40:43], v[76:83], v17\n\t"
    "ds_read_b128 v[72:75], v5 offset:256\n\t"
    "s_waitcnt vmcnt(28)\n\t"
    "v_xad_u32 v213, v213, v253, v252\n\t"
    "v_smfmac_f32_16x16x64_bf16 v[112:115], a[44:47], v[76:83], v17 abid:1\n\t"
    "global_load_dwordx4 a[96:99], v11, s[30:31] offset:0 sc1\n\t"
    "ds_read_b128 v[76:79], v4 offset:2304\n\t"
    "v_lshrrev_b32 v214, 16, v214\n\t"
    "v_smfmac_f32_16x16x64_bf16 v[116:119], a[32:35], v[84:91], v16\n\t"
    "ds_read_b128 v[80:83], v5 offset:2304\n\t"
    "v_xad_u32 v214, v214, v253, v252\n\t"
    "v_smfmac_f32_16x16x64_bf16 v[120:123], a[36:39], v[84:91], v16 abid:1\n\t"
    "v_lshrrev_b32 v215, 16, v215\n\t"
    "v_smfmac_f32_16x16x64_bf16 v[124:127], a[40:43], v[84:91], v17\n\t"
    "global_load_dwordx4 a[100:103], v11, s[30:31] offset:1024 sc1\n\t"
    "v_xad_u32 v215, v215, v253, v252\n\t"
    "v_smfmac_f32_16x16x64_bf16 v[128:131], a[44:47], v[84:91], v17 abid:1\n\t"
    "ds_read_b128 v[84:87], v4 offset:4352\n\t"
    "s_waitcnt lgkmcnt(5)\n\t"
    "v_lshrrev_b32 v216, 16, v216\n\t"
    "v_smfmac_f32_16x16x64_bf16 v[236:239], a[32:35], v[92:99], v16\n\t"
    "ds_read_b128 v[88:91], v5 offset:4352\n\t"
    "v_xad_u32 v216, v216, v253, v252\n\t"
    "v_smfmac_f32_16x16x64_bf16 v[240:243], a[36:39], v[92:99], v16 abid:1\n\t"
    "global_load_dwordx4 a[104:107], v11, s[30:31] offset:2048 sc1\n\t"
    "v_lshrrev_b32 v217, 16, v217\n\t"
    "v_smfmac_f32_16x16x64_bf16 v[244:247], a[40:43], v[92:99], v17\n\t"
    "v_xad_u32 v217, v217, v253, v252\n\t"
    "v_smfmac_f32_16x16x64_bf16 v[248:251], a[44:47], v[92:99], v17 abid:1\n\t"
    "ds_read_b128 v[92:95], v4 offset:6400\n\t"
    "v_lshrrev_b32 v218, 16, v218\n\t"
    "v_smfmac_f32_16x16x64_bf16 v[44:47], a[32:35], v[60:67], v16\n\t"
    "global_load_dwordx4 a[108:111], v11, s[30:31] offset:3072 sc1\n\t"
    "s_add_u32 s30, s30, 4096\n\t"
    "s_addc_u32 s31, s31, 0\n\t"
    "ds_read_b128 v[96:99], v5 offset:6400\n\t"
    "v_xad_u32 v218, v218, v253, v252\n\t"
    "v_smfmac_f32_16x16x64_bf16 v[48:51], a[36:39], v[60:67], v16 abid:1\n\t"
    "v_lshrrev_b32 v219, 16, v219\n\t"
    "v_smfmac_f32_16x16x64_bf16 v[52:55], a[40:43], v[60:67], v17\n\t"
    "v_xad_u32 v219, v219, v253, v252\n\t"
    "v_smfmac_f32_16x16x64_bf16 v[56:59], a[44:47], v[60:67], v17 abid:1\n\t"
    "s_waitcnt lgkmcnt(2)\n\t"
    "s_waitcnt vmcnt(31)\n\t"
    "v_lshrrev_b32 v220, 16, v220\n\t"
    "v_smfmac_f32_16x16x64_bf16 v[100:103], a[48:51], v[68:75], v18\n\t"
    "global_load_dwordx4 a[112:115], v11, s[30:31] offset:0 sc1\n\t"
    "s_waitcnt vmcnt(31)\n\t"
    "v_xad_u32 v220, v220, v253, v252\n\t"
    "v_smfmac_f32_16x16x64_bf16 v[104:107], a[52:55], v[68:75], v18 abid:1\n\t"
    "ds_read_b128 v[60:63], v2 offset:512\n\t"
    "s_waitcnt vmcnt(30)\n\t"
    "v_lshrrev_b32 v221, 16, v221\n\t"
    "v_smfmac_f32_16x16x64_bf16 v[108:111], a[56:59], v[68:75], v19\n\t"
    "global_load_dwordx4 a[116:119], v11, s[30:31] offset:1024 sc1\n\t"
    "s_waitcnt vmcnt(30)\n\t"
    "v_xad_u32 v221, v221, v253, v252\n\t"
    "v_smfmac_f32_16x16x64_bf16 v[112:115], a[60:63], v[68:75], v19 abid:1\n\t"
    "ds_read_b128 v[64:67], v3 offset:512\n\t"
    "v_lshrrev_b32 v222, 16, v222\n\t"
    "v_smfmac_f32_16x16x64_bf16 v[116:119], a[48:51], v[76:83], v18\n\t"
    "ds_read_b128 v[68:71], v2 offset:2560\n\t"
    "v_xad_u32 v222, v222, v253, v252\n\t"
    "v_smfmac_f32_16x16x64_bf16 v[120:123], a[52:55], v[76:83], v18 abid:1\n\t"
    "global_load_dwordx4 a[120:123], v11, s[30:31] offset:2048 sc1\n\t"
    "ds_read_b128 v[72:75], v3 offset:2560\n\t"
    "v_lshrrev_b32 v223, 16, v223\n\t"
    "v_smfmac_f32_16x16x64_bf16 v[124:127], a[56:59], v[76:83], v19\n\t"
    "v_xad_u32 v223, v223, v253, v252\n\t"
    "v_smfmac_f32_16x16x64_bf16 v[128:131], a[60:63], v[76:83], v19 abid:1\n\t"
    "ds_read_b128 v[76:79], v2 offset:4608\n\t"
    "v_lshrrev_b32 v224, 16, v224\n\t"
    "v_smfmac_f32_16x16x64_bf16 v[236:239], a[48:51], v[84:91], v18\n\t"
    "global_load_dwordx4 a[124:127], v11, s[30:31] offset:3072 sc1\n\t"
    "s_add_u32 s30, s30, 4096\n\t"
    "s_addc_u32 s31, s31, 0\n\t"
    "ds_read_b128 v[80:83], v3 offset:4608\n\t"
    "v_xad_u32 v224, v224, v253, v252\n\t"
    "v_smfmac_f32_16x16x64_bf16 v[240:243], a[52:55], v[84:91], v18 abid:1\n\t"
    "v_lshrrev_b32 v225, 16, v225\n\t"
    "v_smfmac_f32_16x16x64_bf16 v[244:247], a[56:59], v[84:91], v19\n\t"
    "v_xad_u32 v225, v225, v253, v252\n\t"
    "v_smfmac_f32_16x16x64_bf16 v[248:251], a[60:63], v[84:91], v19 abid:1\n\t"
    "global_load_dwordx4 v[148:151], v148, s[56:57] sc1\n\t"
    "s_waitcnt lgkmcnt(3)\n\t"
    "v_lshrrev_b32 v226, 16, v226\n\t"
    "v_smfmac_f32_16x16x64_bf16 v[44:47], a[48:51], v[92:99], v18\n\t"
    "ds_read_b128 v[84:87], v2 offset:6656\n\t"
    "v_xad_u32 v226, v226, v253, v252\n\t"
    "v_smfmac_f32_16x16x64_bf16 v[48:51], a[52:55], v[92:99], v18 abid:1\n\t"
    "ds_read_b128 v[88:91], v3 offset:6656\n\t"
    "v_lshrrev_b32 v227, 16, v227\n\t"
    "v_smfmac_f32_16x16x64_bf16 v[52:55], a[56:59], v[92:99], v19\n\t"
    "global_load_dwordx4 v[152:155], v152, s[56:57] sc1\n\t"
    "v_xad_u32 v227, v227, v253, v252\n\t"
    "v_smfmac_f32_16x16x64_bf16 v[56:59], a[60:63], v[92:99], v19 abid:1\n\t"
    "ds_read_b128 v[92:95], v4 offset:512\n\t"
    "s_waitcnt vmcnt(17)\n\t"
    "v_lshrrev_b32 v228, 16, v228\n\t"
    "v_smfmac_f32_16x16x64_bf16 v[100:103], a[64:67], v[60:67], v20\n\t"
    "ds_read_b128 v[96:99], v5 offset:512\n\t"
    "s_waitcnt vmcnt(16)\n\t"
    "v_xad_u32 v228, v228, v253, v252\n\t"
    "v_smfmac_f32_16x16x64_bf16 v[104:107], a[68:71], v[60:67], v20 abid:1\n\t"
    "global_load_dwordx4 v[156:159], v156, s[56:57] sc1\n\t"
    "s_waitcnt vmcnt(16)\n\t"
    "v_lshrrev_b32 v229, 16, v229\n\t"
    "v_smfmac_f32_16x16x64_bf16 v[108:111], a[72:75], v[60:67], v21\n\t"
    "s_waitcnt vmcnt(15)\n\t"
    "v_xad_u32 v229, v229, v253, v252\n\t"
    "v_smfmac_f32_16x16x64_bf16 v[112:115], a[76:79], v[60:67], v21 abid:1\n\t"
    "ds_read_b128 v[60:63], v4 offset:2560\n\t"
    "s_waitcnt lgkmcnt(4)\n\t"
    "v_lshrrev_b32 v230, 16, v230\n\t"
    "v_smfmac_f32_16x16x64_bf16 v[116:119], a[64:67], v[68:75], v20\n\t"
    "global_load_dwordx4 v[160:163], v160, s[56:57] sc1\n\t"
    "ds_read_b128 v[64:67], v5 offset:2560\n\t"
    "v_xad_u32 v230, v230, v253, v252\n\t"
    "v_smfmac_f32_16x16x64_bf16 v[120:123], a[68:71], v[68:75], v20 abid:1\n\t"
    "v_lshrrev_b32 v231, 16, v231\n\t"
    "v_smfmac_f32_16x16x64_bf16 v[124:127], a[72:75], v[68:75], v21\n\t"
    "v_xad_u32 v231, v231, v253, v252\n\t"
    "v_smfmac_f32_16x16x64_bf16 v[128:131], a[76:79], v[68:75], v21 abid:1\n\t"
    "global_load_dwordx4 v[164:167], v164, s[56:57] sc1\n\t"
    "ds_read_b128 v[68:71], v4 offset:4608\n\t"
    "v_lshrrev_b32 v232, 16, v232\n\t"
    "v_smfmac_f32_16x16x64_bf16 v[236:239], a[64:67], v[76:83], v20\n\t"
    "ds_read_b128 v[72:75], v5 offset:4608\n\t"
    "v_xad_u32 v232, v232, v253, v252\n\t"
    "v_smfmac_f32_16x16x64_bf16 v[240:243], a[68:71], v[76:83], v20 abid:1\n\t"
    "v_lshrrev_b32 v233, 16, v233\n\t"
    "v_smfmac_f32_16x16x64_bf16 v[244:247], a[72:75], v[76:83], v21\n\t"
    "global_load_dwordx4 v[168:171], v168, s[56:57] sc1\n\t"
    "v_xad_u32 v233, v233, v253, v252\n\t"
    "v_smfmac_f32_16x16x64_bf16 v[248:251], a[76:79], v[76:83], v21 abid:1\n\t"
    "ds_read_b128 v[76:79], v4 offset:6656\n\t"
    "s_waitcnt lgkmcnt(3)\n\t"
    "v_lshrrev_b32 v234, 16, v234\n\t"
    "v_smfmac_f32_16x16x64_bf16 v[44:47], a[64:67], v[84:91], v20\n\t"
    "ds_read_b128 v[80:83], v5 offset:6656\n\t"
    "v_xad_u32 v234, v234, v253, v252\n\t"
    "v_smfmac_f32_16x16x64_bf16 v[48:51], a[68:71], v[84:91], v20 abid:1\n\t"
    "global_load_dwordx4 v[172:175], v172, s[56:57] sc1\n\t"
    "v_lshrrev_b32 v235, 16, v235\n\t"
    "v_smfmac_f32_16x16x64_bf16 v[52:55], a[72:75], v[84:91], v21\n\t"
    "v_xad_u32 v235, v235, v253, v252\n\t"
    "v_smfmac_f32_16x16x64_bf16 v[56:59], a[76:79], v[84:91], v21 abid:1\n\t"
    "ds_read_b128 v[84:87], v2 offset:768\n\t"
    "s_waitcnt vmcnt(18)\n\t"
    "v_smfmac_f32_16x16x64_bf16 v[100:103], a[80:83], v[92:99], v22\n\t"
    "global_load_dwordx4 v[176:179], v176, s[56:57] sc1\n\t"
    "ds_read_b128 v[88:91], v3 offset:768\n\t"
    "s_waitcnt vmcnt(18)\n\t"
    "v_smfmac_f32_16x16x64_bf16 v[104:107], a[84:87], v[92:99], v22 abid:1\n\t"
    "s_waitcnt vmcnt(17)\n\t"
    "v_smfmac_f32_16x16x64_bf16 v[108:111], a[88:91], v[92:99], v23\n\t"
    "s_waitcnt vmcnt(16)\n\t"
    "v_smfmac_f32_16x16x64_bf16 v[112:115], a[92:95], v[92:99], v23 abid:1\n\t"
    "global_load_dwordx4 v[28:31], v11, s[36:37] offset:0 sc1\n\t"
    "ds_read_b128 v[92:95], v2 offset:2816\n\t"
    "v_smfmac_f32_16x16x64_bf16 v[116:119], a[80:83], v[60:67], v22\n\t"
    "ds_read_b128 v[96:99], v3 offset:2816\n\t"
    "v_smfmac_f32_16x16x64_bf16 v[120:123], a[84:87], v[60:67], v22 abid:1\n\t"
    "v_smfmac_f32_16x16x64_bf16 v[124:127], a[88:91], v[60:67], v23\n\t"
    "global_load_dwordx4 v[32:35], v11, s[36:37] offset:1024 sc1\n\t"
    "v_smfmac_f32_16x16x64_bf16 v[128:131], a[92:95], v[60:67], v23 abid:1\n\t"
    "ds_read_b128 v[60:63], v2 offset:4864\n\t"
    "s_waitcnt lgkmcnt(3)\n\t"
    "v_smfmac_f32_16x16x64_bf16 v[236:239], a[80:83], v[68:75], v22\n\t"
    "ds_read_b128 v[64:67], v3 offset:4864\n\t"
    "v_smfmac_f32_16x16x64_bf16 v[240:243], a[84:87], v[68:75], v22 abid:1\n\t"
    "global_load_dwordx4 v[36:39], v11, s[36:37] offset:2048 sc1\n\t"
    "v_smfmac_f32_16x16x64_bf16 v[244:247], a[88:91], v[68:75], v23\n\t"
    "v_smfmac_f32_16x16x64_bf16 v[248:251], a[92:95], v[68:75], v23 abid:1\n\t"
    "ds_read_b128 v[68:71], v2 offset:6912\n\t"
    "v_smfmac_f32_16x16x64_bf16 v[44:47], a[80:83], v[76:83], v22\n\t"
    "global_load_dwordx4 v[40:43], v11, s[36:37] offset:3072 sc1\n\t"
    "ds_read_b128 v[72:75], v3 offset:6912\n\t"
    "v_smfmac_f32_16x16x64_bf16 v[48:51], a[84:87], v[76:83], v22 abid:1\n\t"
    "v_smfmac_f32_16x16x64_bf16 v[52:55], a[88:91], v[76:83], v23\n\t"
    "v_smfmac_f32_16x16x64_bf16 v[56:59], a[92:95], v[76:83], v23 abid:1\n\t"
    "global_load_dwordx4 a[128:131], v11, s[30:31] offset:0 sc1\n\t"
    "ds_read_b128 v[76:79], v4 offset:768\n\t"
    "s_waitcnt vmcnt(20)\n\t"
    "v_smfmac_f32_16x16x64_bf16 v[100:103], a[96:99], v[84:91], v24\n\t"
    "ds_read_b128 v[80:83], v5 offset:768\n\t"
    "s_waitcnt vmcnt(19)\n\t"
    "v_smfmac_f32_16x16x64_bf16 v[104:107], a[100:103], v[84:91], v24 abid:1\n\t"
    "s_waitcnt vmcnt(18)\n\t"
    "v_smfmac_f32_16x16x64_bf16 v[108:111], a[104:107], v[84:91], v25\n\t"
    "global_load_dwordx4 a[132:135], v11, s[30:31] offset:1024 sc1\n\t"
    "s_waitcnt vmcnt(18)\n\t"
    "v_smfmac_f32_16x16x64_bf16 v[112:115], a[108:111], v[84:91], v25 abid:1\n\t"
    "ds_read_b128 v[84:87], v4 offset:2816\n\t"
    "s_waitcnt lgkmcnt(3)\n\t"
    "v_smfmac_f32_16x16x64_bf16 v[116:119], a[96:99], v[92:99], v24\n\t"
    "ds_read_b128 v[88:91], v5 offset:2816\n\t"
    "v_smfmac_f32_16x16x64_bf16 v[120:123], a[100:103], v[92:99], v24 abid:1\n\t"
    "global_load_dwordx4 a[136:139], v11, s[30:31] offset:2048 sc1\n\t"
    "s_waitcnt vmcnt(14)\n\t"
    "ds_write_b128 v255, v[148:151] offset:0\n\t"
    "ds_read_b32 v148, v254 offset:0\n\t"
    "v_smfmac_f32_16x16x64_bf16 v[124:127], a[104:107], v[92:99], v25\n\t"
    "ds_read_b32 v149, v254 offset:64\n\t"
    "v_smfmac_f32_16x16x64_bf16 v[128:131], a[108:111], v[92:99], v25 abid:1\n\t"
    "ds_read_b128 v[92:95], v4 offset:4864\n\t"
    "v_smfmac_f32_16x16x64_bf16 v[236:239], a[96:99], v[60:67], v24\n\t"
    "global_load_dwordx4 a[140:143], v11, s[30:31] offset:3072 sc1\n\t"
    "s_add_u32 s30, s30, 4096\n\t"
    "s_addc_u32 s31, s31, 0\n\t"
    "ds_read_b128 v[96:99], v5 offset:4864\n\t"
    "v_smfmac_f32_16x16x64_bf16 v[240:243], a[100:103], v[60:67], v24 abid:1\n\t"
    "ds_read_b32 v150, v254 offset:128\n\t"
    "v_smfmac_f32_16x16x64_bf16 v[244:247], a[104:107], v[60:67], v25\n\t"
    "ds_read_b32 v151, v254 offset:192\n\t"
    "v_smfmac_f32_16x16x64_bf16 v[248:251], a[108:111], v[60:67], v25 abid:1\n\t"
    "global_load_dwordx4 a[144:147], v11, s[30:31] offset:0 sc1\n\t"
    "ds_read_b128 v[60:63], v4 offset:6912\n\t"
    "v_smfmac_f32_16x16x64_bf16 v[44:47], a[96:99], v[68:75], v24\n\t"
    "ds_read_b128 v[64:67], v5 offset:6912\n\t"
    "v_smfmac_f32_16x16x64_bf16 v[48:51], a[100:103], v[68:75], v24 abid:1\n\t"
    "s_waitcnt vmcnt(15)\n\t"
    "ds_write_b128 v255, v[152:155] offset:1024\n\t"
    "ds_read_b32 v152, v254 offset:1024\n\t"
    "v_smfmac_f32_16x16x64_bf16 v[52:55], a[104:107], v[68:75], v25\n\t"
    "global_load_dwordx4 a[148:151], v11, s[30:31] offset:1024 sc1\n\t"
    "ds_read_b32 v153, v254 offset:1088\n\t"
    "v_smfmac_f32_16x16x64_bf16 v[56:59], a[108:111], v[68:75], v25 abid:1\n\t"
    "ds_read_b32 v154, v254 offset:1152\n\t"
    "s_waitcnt lgkmcnt(8)\n\t"
    "v_smfmac_f32_16x16x64_bf16 v[100:103], a[112:115], v[76:83], v26\n\t"
    "ds_read_b32 v155, v254 offset:1216\n\t"
    "v_smfmac_f32_16x16x64_bf16 v[104:107], a[116:119], v[76:83], v26 abid:1\n\t"
    "global_load_dwordx4 a[152:155], v11, s[30:31] offset:2048 sc1\n\t"
    "s_waitcnt vmcnt(16)\n\t"
    "ds_write_b128 v255, v[156:159] offset:10240\n\t"
    "ds_read_b32 v156, v254 offset:10240\n\t"
    "v_smfmac_f32_16x16x64_bf16 v[108:111], a[120:123], v[76:83], v27\n\t"
    "ds_read_b32 v157, v254 offset:10304\n\t"
    "v_smfmac_f32_16x16x64_bf16 v[112:115], a[124:127], v[76:83], v27 abid:1\n\t"
    "s_barrier\n\t"
    "ds_read_b128 v[68:71], v2 offset:1024\n\t"
    "v_smfmac_f32_16x16x64_bf16 v[116:119], a[112:115], v[84:91], v26\n\t"
    "ds_read_b128 v[72:75], v3 offset:1024\n\t"
    "global_load_dwordx4 a[156:159], v11, s[30:31] offset:3072 sc1\n\t"
    "s_add_u32 s30, s30, 4096\n\t"
    "s_addc_u32 s31, s31, 0\n\t"
    "v_smfmac_f32_16x16x64_bf16 v[120:123], a[116:119], v[84:91], v26 abid:1\n\t"
    "ds_read_b32 v158, v254 offset:10368\n\t"
    "v_smfmac_f32_16x16x64_bf16 v[124:127], a[120:123], v[84:91], v27\n\t"
    "ds_read_b32 v159, v254 offset:10432\n\t"
    "v_smfmac_f32_16x16x64_bf16 v[128:131], a[124:127], v[84:91], v27 abid:1\n\t"
    "s_waitcnt vmcnt(16)\n\t"
    "ds_write_b128 v255, v[160:163] offset:11264\n\t"
    "ds_read_b32 v160, v254 offset:11264\n\t"
    "global_load_dwordx4 a[160:163], v11, s[30:31] offset:0 sc1\n\t"
    "v_smfmac_f32_16x16x64_bf16 v[236:239], a[112:115], v[92:99], v26\n\t"
    "ds_read_b32 v161, v254 offset:11328\n\t"
    "v_smfmac_f32_16x16x64_bf16 v[240:243], a[116:119], v[92:99], v26 abid:1\n\t"
    "ds_read_b32 v162, v254 offset:11392\n\t"
    "ds_read_b32 v163, v254 offset:11456\n\t"
    "ds_read_b64 v[76:77], v196 offset:0\n\t"
    "v_smfmac_f32_16x16x64_bf16 v[244:247], a[120:123], v[92:99], v27\n\t"
    "ds_read_b64 v[78:79], v197 offset:0\n\t"
    "ds_read_b64 v[80:81], v198 offset:0\n\t"
    "ds_read_b64 v[82:83], v199 offset:0\n\t"
    "ds_read_b64 v[84:85], v204 offset:0\n\t"
    "v_smfmac_f32_16x16x64_bf16 v[248:251], a[124:127], v[92:99], v27 abid:1\n\t"
    "ds_read_b64 v[86:87], v205 offset:0\n\t"
    "ds_read_b64 v[88:89], v206 offset:0\n\t"
    "ds_read_b64 v[90:91], v207 offset:0\n\t"
    "s_waitcnt vmcnt(16)\n\t"
    "ds_write_b128 v255, v[164:167] offset:20480\n\t"
    "ds_read_b32 v164, v254 offset:20480\n\t"
    "s_waitcnt lgkmcnt(15)\n\t"
    "v_smfmac_f32_16x16x64_bf16 v[44:47], a[112:115], v[60:67], v26\n\t"
    "ds_read_b32 v165, v254 offset:20544\n\t"
    "ds_read_b32 v166, v254 offset:20608\n\t"
    "ds_read_b32 v167, v254 offset:20672\n\t"
    "ds_read_b128 v[92:95], v2 offset:3072\n\t"
    "global_load_dwordx4 a[164:167], v11, s[30:31] offset:1024 sc1\n\t"
    "v_smfmac_f32_16x16x64_bf16 v[48:51], a[116:119], v[60:67], v26 abid:1\n\t"
    "ds_read_b128 v[96:99], v3 offset:3072\n\t"
    "global_load_dwordx4 a[168:171], v11, s[30:31] offset:2048 sc1\n\t"
    "v_smfmac_f32_16x16x64_bf16 v[52:55], a[120:123], v[60:67], v27\n\t"
    "s_waitcnt vmcnt(17)\n\t"
    "ds_write_b128 v255, v[168:171] offset:21504\n\t"
    "ds_read_b32 v168, v254 offset:21504\n\t"
    "v_smfmac_f32_16x16x64_bf16 v[56:59], a[124:127], v[60:67], v27 abid:1\n\t"
    "ds_read_b32 v169, v254 offset:21568\n\t"
    "ds_read_b32 v170, v254 offset:21632\n\t"
    "ds_read_b32 v171, v254 offset:21696\n\t"
    "s_waitcnt vmcnt(10)\n\t"
    "v_smfmac_f32_16x16x64_bf16 v[100:103], a[128:131], v[68:75], v28\n\t"
    "ds_read_b64 v[60:61], v212 offset:0\n\t"
    "global_load_dwordx4 a[172:175], v11, s[30:31] offset:3072 sc1\n\t"
    "s_add_u32 s30, s30, 4096\n\t"
    "s_addc_u32 s31, s31, 0\n\t"
    "s_waitcnt vmcnt(10)\n\t"
    "v_smfmac_f32_16x16x64_bf16 v[104:107], a[132:135], v[68:75], v28 abid:1\n\t"
    "ds_read_b64 v[62:63], v213 offset:0\n\t"
    "ds_read_b64 v[64:65], v214 offset:0\n\t"
    "ds_read_b64 v[66:67], v215 offset:0\n\t"
    "ds_write_b128 v255, v[172:175] offset:30720\n\t"
    "ds_read_b32 v172, v254 offset:30720\n\t"
    "s_waitcnt vmcnt(9)\n\t"
    "v_smfmac_f32_16x16x64_bf16 v[108:111], a[136:139], v[68:75], v29\n\t"
    "ds_read_b32 v173, v254 offset:30784\n\t"
    "ds_read_b32 v174, v254 offset:30848\n\t"
    "ds_read_b32 v175, v254 offset:30912\n\t"
    "s_waitcnt vmcnt(8)\n\t"
    "v_smfmac_f32_16x16x64_bf16 v[112:115], a[140:143], v[68:75], v29 abid:1\n\t"
    "ds_read_b64 v[68:69], v220 offset:0\n\t"
    "global_load_dwordx4 a[176:179], v11, s[30:31] offset:0 sc1\n\t"
    "s_waitcnt lgkmcnt(15)\n\t"
    "v_smfmac_f32_16x16x64_bf16 v[100:103], v[148:151], v[76:83], v10\n\t"
    "ds_write_b128 v255, v[176:179] offset:31744\n\t"
    "ds_read_b32 v176, v254 offset:31744\n\t"
    "v_smfmac_f32_16x16x64_bf16 v[104:107], v[152:155], v[84:91], v10\n\t"
    "ds_read_b64 v[70:71], v221 offset:0\n\t"
    "ds_read_b32 v177, v254 offset:31808\n\t"
    "ds_read_b64 v[72:73], v222 offset:0\n\t"
    "ds_read_b32 v178, v254 offset:31872\n\t"
    "ds_read_b64 v[74:75], v223 offset:0\n\t"
    "ds_read_b32 v179, v254 offset:31936\n\t"
    "ds_read_b128 v[76:79], v2 offset:5120\n\t"
    "v_smfmac_f32_16x16x64_bf16 v[116:119], a[128:131], v[92:99], v28\n\t"
    "ds_read_b128 v[80:83], v3 offset:5120\n\t"
    "ds_read_b64 v[84:85], v200 offset:0\n\t"
    "global_load_dwordx4 a[180:183], v11, s[30:31] offset:1024 sc1\n\t"
    "v_smfmac_f32_16x16x64_bf16 v[120:123], a[132:135], v[92:99], v28 abid:1\n\t"
    "ds_read_b64 v[86:87], v201 offset:0\n\t"
    "ds_read_b64 v[88:89], v202 offset:0\n\t"
    "ds_read_b64 v[90:91], v203 offset:0\n\t"
    "v_smfmac_f32_16x16x64_bf16 v[124:127], a[136:139], v[92:99], v29\n\t"
    "v_smfmac_f32_16x16x64_bf16 v[128:131], a[140:143], v[92:99], v29 abid:1\n\t"
    "ds_read_b64 v[92:93], v208 offset:0\n\t"
    "global_load_dwordx4 a[184:187], v11, s[30:31] offset:2048 sc1\n\t"
    "s_waitcnt lgkmcnt(15)\n\t"
    "v_smfmac_f32_16x16x64_bf16 v[108:111], v[156:159], v[60:67], v10\n\t"
    "ds_read_b64 v[94:95], v209 offset:0\n\t"
    "s_waitcnt lgkmcnt(9)\n\t"
    "v_smfmac_f32_16x16x64_bf16 v[112:115], v[160:163], v[68:75], v10\n\t"
    "ds_read_b64 v[96:97], v210 offset:0\n\t"
    "ds_read_b64 v[98:99], v211 offset:0\n\t"
    "ds_read_b128 v[60:63], v2 offset:7168\n\t"
    "s_waitcnt lgkmcnt(9)\n\t"
    "v_smfmac_f32_16x16x64_bf16 v[236:239], a[128:131], v[76:83], v28\n\t"
    "ds_read_b128 v[64:67], v3 offset:7168\n\t"
    "ds_read_b64 v[68:69], v216 offset:0\n\t"
    "global_load_dwordx4 a[188:191], v11, s[30:31] offset:3072 sc1\n\t"
    "s_add_u32 s30, s30, 4096\n\t"
    "s_addc_u32 s31, s31, 0\n\t"
    "v_smfmac_f32_16x16x64_bf16 v[240:243], a[132:135], v[76:83], v28 abid:1\n\t"
    "ds_read_b64 v[70:71], v217 offset:0\n\t"
    "ds_read_b64 v[72:73], v218 offset:0\n\t"
    "ds_read_b64 v[74:75], v219 offset:0\n\t"
    "v_smfmac_f32_16x16x64_bf16 v[244:247], a[136:139], v[76:83], v29\n\t"
    "v_smfmac_f32_16x16x64_bf16 v[248:251], a[140:143], v[76:83], v29 abid:1\n\t"
    "ds_read_b64 v[76:77], v224 offset:0\n\t"
    "global_load_dwordx4 v[180:183], v180, s[56:57] sc1\n\t"
    "s_waitcnt lgkmcnt(11)\n\t"
    "v_smfmac_f32_16x16x64_bf16 v[100:103], v[164:167], v[84:91], v10\n\t"
    "ds_read_b64 v[78:79], v225 offset:0\n\t"
    "s_waitcnt lgkmcnt(8)\n\t"
    "v_smfmac_f32_16x16x64_bf16 v[104:107], v[168:171], v[92:99], v10\n\t"
    "ds_read_b64 v[80:81], v226 offset:0\n\t"
    "ds_read_b64 v[82:83], v227 offset:0\n\t"
    "ds_read_b128 v[84:87], v4 offset:1024\n\t"
    "s_waitcnt lgkmcnt(9)\n\t"
    "v_smfmac_f32_16x16x64_bf16 v[44:47], a[128:131], v[60:67], v28\n\t"
    "ds_read_b128 v[88:91], v5 offset:1024\n\t"
    "ds_read_b64 v[92:93], v228 offset:0\n\t"
    "global_load_dwordx4 v[184:187], v184, s[56:57] sc1\n\t"
    "v_smfmac_f32_16x16x64_bf16 v[48:51], a[132:135], v[60:67], v28 abid:1\n\t"
    "ds_read_b64 v[94:95], v229 offset:0\n\t"
    "ds_read_b64 v[96:97], v230 offset:0\n\t"
    "ds_read_b64 v[98:99], v231 offset:0\n\t"
    "v_smfmac_f32_16x16x64_bf16 v[52:55], a[136:139], v[60:67], v29\n\t"
    "v_smfmac_f32_16x16x64_bf16 v[56:59], a[140:143], v[60:67], v29 abid:1\n\t"
    "ds_read_b64 v[60:61], v232 offset:0\n\t"
    "global_load_dwordx4 v[188:191], v188, s[56:57] sc1\n\t"
    "s_waitcnt lgkmcnt(11)\n\t"
    "v_smfmac_f32_16x16x64_bf16 v[108:111], v[172:175], v[68:75], v10\n\t"
    "ds_read_b64 v[62:63], v233 offset:0\n\t"
    "s_waitcnt lgkmcnt(8)\n\t"
    "v_smfmac_f32_16x16x64_bf16 v[112:115], v[176:179], v[76:83], v10\n\t"
    "ds_read_b64 v[64:65], v234 offset:0\n\t"
    "ds_read_b64 v[66:67], v235 offset:0\n\t"
    "ds_read_b128 v[68:71], v4 offset:3072\n\t"
    "s_waitcnt lgkmcnt(9)\n\t"
    "s_waitcnt vmcnt(14)\n\t"
    "v_smfmac_f32_16x16x64_bf16 v[100:103], a[144:147], v[84:91], v30\n\t"
    "ds_read_b128 v[72:75], v5 offset:3072\n\t"
    "global_load_dwordx4 v[192:195], v192, s[56:57] sc1\n\t"
    "s_waitcnt vmcnt(14)\n\t"
    "v_smfmac_f32_16x16x64_bf16 v[104:107], a[148:151], v[84:91], v30 abid:1\n\t"
    "ds_read_b64 v[76:77], v196 offset:2048\n\t"
    "s_waitcnt vmcnt(13)\n\t"
    "v_smfmac_f32_16x16x64_bf16 v[108:111], a[152:155], v[84:91], v31\n\t"
    "ds_read_b64 v[78:79], v197 offset:2048\n\t"
    "ds_read_b64 v[80:81], v198 offset:2048\n\t"
    "ds_read_b64 v[82:83], v199 offset:2048\n\t"
    "s_waitcnt vmcnt(12)\n\t"
    "v_smfmac_f32_16x16x64_bf16 v[112:115], a[156:159], v[84:91], v31 abid:1\n\t"
    "s_waitcnt vmcnt(3)\n\t"
    "ds_write_b128 v255, v[180:183] offset:0\n\t"
    "ds_read_b32 v180, v254 offset:0\n\t"
    "ds_read_b32 v181, v254 offset:64\n\t"
    "ds_read_b32 v182, v254 offset:128\n\t"
    "ds_read_b32 v183, v254 offset:192\n\t"
    "ds_read_b64 v[84:85], v204 offset:2048\n\t"
    "global_load_dwordx4 a[192:195], v11, s[30:31] offset:0 sc1\n\t"
    "s_waitcnt lgkmcnt(15)\n\t"
    "s_waitcnt lgkmcnt(1)\n\t"
    "v_smfmac_f32_16x16x64_bf16 v[100:103], v[180:183], v[92:99], v10\n\t"
    "s_waitcnt vmcnt(3)\n\t"
    "ds_write_b128 v255, v[184:187] offset:1024\n\t"
    "ds_read_b32 v184, v254 offset:1024\n\t"
    "ds_read_b32 v185, v254 offset:1088\n\t"
    "ds_read_b32 v186, v254 offset:1152\n\t"
    "ds_read_b32 v187, v254 offset:1216\n\t"
    "ds_read_b64 v[86:87], v205 offset:2048\n\t"
    "s_waitcnt lgkmcnt(1)\n\t"
    "v_smfmac_f32_16x16x64_bf16 v[104:107], v[184:187], v[92:99], v10\n\t"
    "s_waitcnt vmcnt(2)\n\t"
    "ds_write_b128 v255, v[188:191] offset:10240\n\t"
    "ds_read_b32 v188, v254 offset:10240\n\t"
    "ds_read_b32 v189, v254 offset:10304\n\t"
    "ds_read_b32 v190, v254 offset:10368\n\t"
    "ds_read_b32 v191, v254 offset:10432\n\t"
    "ds_read_b64 v[88:89], v206 offset:2048\n\t"
    "ds_read_b64 v[90:91], v207 offset:2048\n\t"
    "s_waitcnt lgkmcnt(2)\n\t"
    "v_smfmac_f32_16x16x64_bf16 v[108:111], v[188:191], v[60:67], v10\n\t"
    "s_waitcnt vmcnt(1)\n\t"
    "ds_write_b128 v255, v[192:195] offset:11264\n\t"
    "ds_read_b32 v192, v254 offset:11264\n\t"
    "ds_read_b32 v193, v254 offset:11328\n\t"
    "ds_read_b32 v194, v254 offset:11392\n\t"
    "ds_read_b32 v195, v254 offset:11456\n\t"
    "ds_read_b128 v[92:95], v4 offset:5120\n\t"
    "global_load_dwordx4 a[196:199], v11, s[30:31] offset:1024 sc1\n\t"
    "s_waitcnt lgkmcnt(1)\n\t"
    "v_smfmac_f32_16x16x64_bf16 v[112:115], v[192:195], v[60:67], v10\n\t"
    "ds_read_b128 v[96:99], v5 offset:5120\n\t"
    "v_smfmac_f32_16x16x64_bf16 v[116:119], a[144:147], v[68:75], v30\n\t"
    "ds_read_b64 v[60:61], v212 offset:2048\n\t"
    "v_smfmac_f32_16x16x64_bf16 v[120:123], a[148:151], v[68:75], v30 abid:1\n\t"
    "ds_read_b64 v[62:63], v213 offset:2048\n\t"
    "ds_read_b64 v[64:65], v214 offset:2048\n\t"
    "ds_read_b64 v[66:67], v215 offset:2048\n\t"
    "v_smfmac_f32_16x16x64_bf16 v[124:127], a[152:155], v[68:75], v31\n\t"
    "global_load_dwordx4 a[200:203], v11, s[30:31] offset:2048 sc1\n\t"
    "v_smfmac_f32_16x16x64_bf16 v[128:131], a[156:159], v[68:75], v31 abid:1\n\t"
    "ds_read_b64 v[68:69], v220 offset:2048\n\t"
    "v_smfmac_f32_16x16x64_bf16 v[116:119], v[148:151], v[76:83], v10\n\t"
    "ds_read_b64 v[70:71], v221 offset:2048\n\t"
    "global_load_dwordx4 a[204:207], v11, s[30:31] offset:3072 sc1\n\t"
    "s_add_u32 s30, s30, 4096\n\t"
    "s_addc_u32 s31, s31, 0\n\t"
    "v_smfmac_f32_16x16x64_bf16 v[120:123], v[152:155], v[84:91], v10\n\t"
    "ds_read_b64 v[72:73], v222 offset:2048\n\t"
    "ds_read_b64 v[74:75], v223 offset:2048\n\t"
    "ds_read_b128 v[76:79], v2 offset:1280\n\t"
    "s_waitcnt lgkmcnt(9)\n\t"
    "v_smfmac_f32_16x16x64_bf16 v[236:239], a[144:147], v[92:99], v30\n\t"
    "ds_read_b128 v[80:83], v3 offset:1280\n\t"
    "ds_read_b64 v[84:85], v200 offset:2048\n\t"
    "v_smfmac_f32_16x16x64_bf16 v[240:243], a[148:151], v[92:99], v30 abid:1\n\t"
    "ds_read_b64 v[86:87], v201 offset:2048\n\t"
    "ds_read_b64 v[88:89], v202 offset:2048\n\t"
    "ds_read_b64 v[90:91], v203 offset:2048\n\t"
    "v_smfmac_f32_16x16x64_bf16 v[244:247], a[152:155], v[92:99], v31\n\t"
    "global_load_dwordx4 a[208:211], v11, s[30:31] offset:0 sc1\n\t"
    "v_smfmac_f32_16x16x64_bf16 v[248:251], a[156:159], v[92:99], v31 abid:1\n\t"
    "ds_read_b64 v[92:93], v208 offset:2048\n\t"
    "s_waitcnt lgkmcnt(11)\n\t"
    "v_smfmac_f32_16x16x64_bf16 v[124:127], v[156:159], v[60:67], v10\n\t"
    "ds_read_b64 v[94:95], v209 offset:2048\n\t"
    "global_load_dwordx4 a[212:215], v11, s[30:31] offset:1024 sc1\n\t"
    "s_waitcnt lgkmcnt(8)\n\t"
    "v_smfmac_f32_16x16x64_bf16 v[128:131], v[160:163], v[68:75], v10\n\t"
    "ds_read_b64 v[96:97], v210 offset:2048\n\t"
    "ds_read_b64 v[98:99], v211 offset:2048\n\t"
    "ds_read_b128 v[60:63], v2 offset:3328\n\t"
    "s_waitcnt lgkmcnt(9)\n\t"
    "v_smfmac_f32_16x16x64_bf16 v[100:103], a[160:163], v[76:83], v32\n\t"
    "ds_read_b128 v[64:67], v3 offset:3328\n\t"
    "ds_read_b64 v[68:69], v216 offset:2048\n\t"
    "v_smfmac_f32_16x16x64_bf16 v[104:107], a[164:167], v[76:83], v32 abid:1\n\t"
    "ds_read_b64 v[70:71], v217 offset:2048\n\t"
    "ds_read_b64 v[72:73], v218 offset:2048\n\t"
    "ds_read_b64 v[74:75], v219 offset:2048\n\t"
    "v_smfmac_f32_16x16x64_bf16 v[108:111], a[168:171], v[76:83], v33\n\t"
    "global_load_dwordx4 a[216:219], v11, s[30:31] offset:2048 sc1\n\t"
    "v_smfmac_f32_16x16x64_bf16 v[112:115], a[172:175], v[76:83], v33 abid:1\n\t"
    "ds_read_b64 v[76:77], v224 offset:2048\n\t"
    "s_waitcnt lgkmcnt(11)\n\t"
    "v_smfmac_f32_16x16x64_bf16 v[116:119], v[164:167], v[84:91], v10\n\t"
    "ds_read_b64 v[78:79], v225 offset:2048\n\t"
    "global_load_dwordx4 a[220:223], v11, s[30:31] offset:3072 sc1\n\t"
    "s_add_u32 s30, s30, 4096\n\t"
    "s_addc_u32 s31, s31, 0\n\t"
    "s_waitcnt lgkmcnt(8)\n\t"
    "v_smfmac_f32_16x16x64_bf16 v[120:123], v[168:171], v[92:99], v10\n\t"
    "ds_read_b64 v[80:81], v226 offset:2048\n\t"
    "ds_read_b64 v[82:83], v227 offset:2048\n\t"
    "ds_read_b128 v[84:87], v2 offset:5376\n\t"
    "s_waitcnt lgkmcnt(9)\n\t"
    "v_smfmac_f32_16x16x64_bf16 v[116:119], a[160:163], v[60:67], v32\n\t"
    "ds_read_b128 v[88:91], v3 offset:5376\n\t"
    "ds_read_b64 v[92:93], v228 offset:2048\n\t"
    "v_smfmac_f32_16x16x64_bf16 v[120:123], a[164:167], v[60:67], v32 abid:1\n\t"
    "ds_read_b64 v[94:95], v229 offset:2048\n\t"
    "ds_read_b64 v[96:97], v230 offset:2048\n\t"
    "ds_read_b64 v[98:99], v231 offset:2048\n\t"
    "v_smfmac_f32_16x16x64_bf16 v[124:127], a[168:171], v[60:67], v33\n\t"
    "global_load_dwordx4 a[224:227], v11, s[30:31] offset:0 sc1\n\t"
    "v_smfmac_f32_16x16x64_bf16 v[128:131], a[172:175], v[60:67], v33 abid:1\n\t"
    "ds_read_b64 v[60:61], v232 offset:2048\n\t"
    "s_waitcnt lgkmcnt(11)\n\t"
    "v_smfmac_f32_16x16x64_bf16 v[124:127], v[172:175], v[68:75], v10\n\t"
    "ds_read_b64 v[62:63], v233 offset:2048\n\t"
    "global_load_dwordx4 a[228:231], v11, s[30:31] offset:1024 sc1\n\t"
    "s_waitcnt lgkmcnt(8)\n\t"
    "v_smfmac_f32_16x16x64_bf16 v[128:131], v[176:179], v[76:83], v10\n\t"
    "ds_read_b64 v[64:65], v234 offset:2048\n\t"
    "ds_read_b64 v[66:67], v235 offset:2048\n\t"
    "ds_read_b128 v[68:71], v4 offset:1280\n\t"
    "s_waitcnt lgkmcnt(9)\n\t"
    "v_smfmac_f32_16x16x64_bf16 v[236:239], a[160:163], v[84:91], v32\n\t"
    "ds_read_b128 v[72:75], v5 offset:1280\n\t"
    "v_smfmac_f32_16x16x64_bf16 v[240:243], a[164:167], v[84:91], v32 abid:1\n\t"
    "ds_read_b64 v[76:77], v196 offset:4096\n\t"
    "global_load_dwordx4 a[232:235], v11, s[30:31] offset:2048 sc1\n\t"
    "v_smfmac_f32_16x16x64_bf16 v[244:247], a[168:171], v[84:91], v33\n\t"
    "ds_read_b64 v[78:79], v197 offset:4096\n\t"
    "ds_read_b64 v[80:81], v198 offset:4096\n\t"
    "ds_read_b64 v[82:83], v199 offset:4096\n\t"
    "v_smfmac_f32_16x16x64_bf16 v[248:251], a[172:175], v[84:91], v33 abid:1\n\t"
    "ds_read_b64 v[84:85], v204 offset:4096\n\t"
    "s_waitcnt lgkmcnt(11)\n\t"
    "v_smfmac_f32_16x16x64_bf16 v[116:119], v[180:183], v[92:99], v10\n\t"
    "ds_read_b64 v[86:87], v205 offset:4096\n\t"
    "global_load_dwordx4 a[236:239], v11, s[30:31] offset:3072 sc1\n\t"
    "s_add_u32 s30, s30, 4096\n\t"
    "s_addc_u32 s31, s31, 0\n\t"
    "v_smfmac_f32_16x16x64_bf16 v[120:123], v[184:187], v[92:99], v10\n\t"
    "ds_read_b64 v[88:89], v206 offset:4096\n\t"
    "ds_read_b64 v[90:91], v207 offset:4096\n\t"
    "s_waitcnt lgkmcnt(10)\n\t"
    "v_smfmac_f32_16x16x64_bf16 v[124:127], v[188:191], v[60:67], v10\n\t"
    "ds_read_b128 v[92:95], v4 offset:3328\n\t"
    "v_smfmac_f32_16x16x64_bf16 v[128:131], v[192:195], v[60:67], v10\n\t"
    "ds_read_b128 v[96:99], v5 offset:3328\n\t"
    "global_load_dwordx4 a[240:243], v11, s[30:31] offset:0 sc1\n\t"
    "s_waitcnt lgkmcnt(10)\n\t"
    "v_smfmac_f32_16x16x64_bf16 v[100:103], a[176:179], v[68:75], v34\n\t"
    "ds_read_b64 v[60:61], v212 offset:4096\n\t"
    "v_smfmac_f32_16x16x64_bf16 v[104:107], a[180:183], v[68:75], v34 abid:1\n\t"
    "ds_read_b64 v[62:63], v213 offset:4096\n\t"
    "ds_read_b64 v[64:65], v214 offset:4096\n\t"
    "ds_read_b64 v[66:67], v215 offset:4096\n\t"
    "v_smfmac_f32_16x16x64_bf16 v[108:111], a[184:187], v[68:75], v35\n\t"
    "global_load_dwordx4 a[244:247], v11, s[30:31] offset:1024 sc1\n\t"
    "v_smfmac_f32_16x16x64_bf16 v[112:115], a[188:191], v[68:75], v35 abid:1\n\t"
    "ds_read_b64 v[68:69], v220 offset:4096\n\t"
    "s_waitcnt lgkmcnt(9)\n\t"
    "v_smfmac_f32_16x16x64_bf16 v[236:239], v[148:151], v[76:83], v10\n\t"
    "ds_read_b64 v[70:71], v221 offset:4096\n\t"
    "s_waitcnt lgkmcnt(8)\n\t"
    "v_smfmac_f32_16x16x64_bf16 v[240:243], v[152:155], v[84:91], v10\n\t"
    "ds_read_b64 v[72:73], v222 offset:4096\n\t"
    "ds_read_b64 v[74:75], v223 offset:4096\n\t"
    "ds_read_b128 v[76:79], v4 offset:5376\n\t"
    "s_waitcnt lgkmcnt(9)\n\t"
    "v_smfmac_f32_16x16x64_bf16 v[116:119], a[176:179], v[92:99], v34\n\t"
    "ds_read_b128 v[80:83], v5 offset:5376\n\t"
    "ds_read_b64 v[84:85], v200 offset:4096\n\t"
    "global_load_dwordx4 a[248:251], v11, s[30:31] offset:2048 sc1\n\t"
    "v_smfmac_f32_16x16x64_bf16 v[120:123], a[180:183], v[92:99], v34 abid:1\n\t"
    "ds_read_b64 v[86:87], v201 offset:4096\n\t"
    "ds_read_b64 v[88:89], v202 offset:4096\n\t"
    "ds_read_b64 v[90:91], v203 offset:4096\n\t"
    "v_smfmac_f32_16x16x64_bf16 v[124:127], a[184:187], v[92:99], v35\n\t"
    "global_load_dwordx4 a[252:255], v11, s[30:31] offset:3072 sc1\n\t"
    "s_add_u32 s30, s30, 4096\n\t"
    "s_addc_u32 s31, s31, 0\n\t"
    "v_smfmac_f32_16x16x64_bf16 v[128:131], a[188:191], v[92:99], v35 abid:1\n\t"
    "ds_read_b64 v[92:93], v208 offset:4096\n\t"
    "s_waitcnt lgkmcnt(11)\n\t"
    "v_smfmac_f32_16x16x64_bf16 v[244:247], v[156:159], v[60:67], v10\n\t"
    "ds_read_b64 v[94:95], v209 offset:4096\n\t"
    "s_waitcnt lgkmcnt(8)\n\t"
    "v_smfmac_f32_16x16x64_bf16 v[248:251], v[160:163], v[68:75], v10\n\t"
    "ds_read_b64 v[96:97], v210 offset:4096\n\t"
    "ds_read_b64 v[98:99], v211 offset:4096\n\t"
    "ds_read_b128 v[60:63], v2 offset:1536\n\t"
    "s_waitcnt lgkmcnt(9)\n\t"
    "v_smfmac_f32_16x16x64_bf16 v[236:239], a[176:179], v[76:83], v34\n\t"
    "ds_read_b128 v[64:67], v3 offset:1536\n\t"
    "ds_read_b64 v[68:69], v216 offset:4096\n\t"
    "s_add_u32 m0, s17, 8192\n\t"
    "s_nop 0\n\t"
    "buffer_load_dwordx4 v6, s[8:11], s16 offen offset:0  lds\n\t"
    "v_smfmac_f32_16x16x64_bf16 v[240:243], a[180:183], v[76:83], v34 abid:1\n\t"
    "ds_read_b64 v[70:71], v217 offset:4096\n\t"
    "ds_read_b64 v[72:73], v218 offset:4096\n\t"
    "ds_read_b64 v[74:75], v219 offset:4096\n\t"
    "v_smfmac_f32_16x16x64_bf16 v[244:247], a[184:187], v[76:83], v35\n\t"
    "buffer_load_dwordx4 v6, s[8:11], s16 offen offset:1024  lds\n\t"
    "v_smfmac_f32_16x16x64_bf16 v[248:251], a[188:191], v[76:83], v35 abid:1\n\t"
    "ds_read_b64 v[76:77], v224 offset:4096\n\t"
    "s_waitcnt lgkmcnt(11)\n\t"
    "v_smfmac_f32_16x16x64_bf16 v[236:239], v[164:167], v[84:91], v10\n\t"
    "ds_read_b64 v[78:79], v225 offset:4096\n\t"
    "s_waitcnt lgkmcnt(8)\n\t"
    "v_smfmac_f32_16x16x64_bf16 v[240:243], v[168:171], v[92:99], v10\n\t"
    "ds_read_b64 v[80:81], v226 offset:4096\n\t"
    "ds_read_b64 v[82:83], v227 offset:4096\n\t"
    "ds_read_b128 v[84:87], v2 offset:3584\n\t"
    "s_waitcnt lgkmcnt(9)\n\t"
    "s_waitcnt vmcnt(17)\n\t"
    "v_smfmac_f32_16x16x64_bf16 v[100:103], a[192:195], v[60:67], v36\n\t"
    "ds_read_b128 v[88:91], v3 offset:3584\n\t"
    "ds_read_b64 v[92:93], v228 offset:4096\n\t"
    "s_waitcnt vmcnt(16)\n\t"
    "v_smfmac_f32_16x16x64_bf16 v[104:107], a[196:199], v[60:67], v36 abid:1\n\t"
    "ds_read_b64 v[94:95], v229 offset:4096\n\t"
    "ds_read_b64 v[96:97], v230 offset:4096\n\t"
    "ds_read_b64 v[98:99], v231 offset:4096\n\t"
    "s_add_u32 m0, s17, 18432\n\t"
    "s_nop 0\n\t"
    "buffer_load_dwordx4 v7, s[8:11], s16 offen offset:0  lds\n\t"
    "s_waitcnt vmcnt(16)\n\t"
    "v_smfmac_f32_16x16x64_bf16 v[108:111], a[200:203], v[60:67], v37\n\t"
    "buffer_load_dwordx4 v7, s[8:11], s16 offen offset:1024  lds\n\t"
    "s_waitcnt vmcnt(16)\n\t"
    "v_smfmac_f32_16x16x64_bf16 v[112:115], a[204:207], v[60:67], v37 abid:1\n\t"
    "ds_read_b64 v[60:61], v232 offset:4096\n\t"
    "s_waitcnt lgkmcnt(11)\n\t"
    "v_smfmac_f32_16x16x64_bf16 v[244:247], v[172:175], v[68:75], v10\n\t"
    "ds_read_b64 v[62:63], v233 offset:4096\n\t"
    "s_waitcnt lgkmcnt(8)\n\t"
    "v_smfmac_f32_16x16x64_bf16 v[248:251], v[176:179], v[76:83], v10\n\t"
    "ds_read_b64 v[64:65], v234 offset:4096\n\t"
    "ds_read_b64 v[66:67], v235 offset:4096\n\t"
    "ds_read_b128 v[68:71], v2 offset:5632\n\t"
    "s_waitcnt lgkmcnt(9)\n\t"
    "v_smfmac_f32_16x16x64_bf16 v[116:119], a[192:195], v[84:91], v36\n\t"
    "ds_read_b128 v[72:75], v3 offset:5632\n\t"
    "s_add_u32 m0, s17, 28672\n\t"
    "s_nop 0\n\t"
    "buffer_load_dwordx4 v8, s[8:11], s16 offen offset:0  lds\n\t"
    "v_smfmac_f32_16x16x64_bf16 v[120:123], a[196:199], v[84:91], v36 abid:1\n\t"
    "ds_read_b128 v[76:79], v4 offset:1536\n\t"
    "v_smfmac_f32_16x16x64_bf16 v[124:127], a[200:203], v[84:91], v37\n\t"
    "ds_read_b128 v[80:83], v5 offset:1536\n\t"
    "buffer_load_dwordx4 v8, s[8:11], s16 offen offset:1024  lds\n\t"
    "v_smfmac_f32_16x16x64_bf16 v[128:131], a[204:207], v[84:91], v37 abid:1\n\t"
    "ds_read_b128 v[84:87], v2 offset:1792\n\t"
    "s_waitcnt lgkmcnt(9)\n\t"
    "v_smfmac_f32_16x16x64_bf16 v[236:239], v[180:183], v[92:99], v10\n\t"
    "ds_read_b128 v[88:91], v3 offset:1792\n\t"
    "v_smfmac_f32_16x16x64_bf16 v[240:243], v[184:187], v[92:99], v10\n\t"
    "s_add_u32 m0, s17, 38912\n\t"
    "s_nop 0\n\t"
    "buffer_load_dwordx4 v9, s[8:11], s16 offen offset:0  lds\n\t"
    "ds_read_b128 v[92:95], v4 offset:1792\n\t"
    "s_waitcnt lgkmcnt(7)\n\t"
    "v_smfmac_f32_16x16x64_bf16 v[244:247], v[188:191], v[60:67], v10\n\t"
    "ds_read_b128 v[96:99], v5 offset:1792\n\t"
    "v_smfmac_f32_16x16x64_bf16 v[248:251], v[192:195], v[60:67], v10\n\t"
    "ds_read_b128 v[60:63], v4 offset:3584\n\t"
    "s_waitcnt lgkmcnt(7)\n\t"
    "v_smfmac_f32_16x16x64_bf16 v[236:239], a[192:195], v[68:75], v36\n\t"
    "buffer_load_dwordx4 v9, s[8:11], s16 offen offset:1024  lds\n\t"
    "s_add_u32 s52, s52, 32768\n\t"
    "s_xor_b32 s16, s52, s53\n\t"
    "ds_read_b128 v[64:67], v5 offset:3584\n\t"
    "v_smfmac_f32_16x16x64_bf16 v[240:243], a[196:199], v[68:75], v36 abid:1\n\t"
    "v_smfmac_f32_16x16x64_bf16 v[244:247], a[200:203], v[68:75], v37\n\t"
    "v_smfmac_f32_16x16x64_bf16 v[248:251], a[204:207], v[68:75], v37 abid:1\n\t"
    "ds_read_b128 v[68:71], v2 offset:3840\n\t"
    "s_waitcnt lgkmcnt(5)\n\t"
    "s_waitcnt vmcnt(19)\n\t"
    "v_smfmac_f32_16x16x64_bf16 v[100:103], a[208:211], v[76:83], v38\n\t"
    "ds_read_b128 v[72:75], v3 offset:3840\n\t"
    "s_waitcnt vmcnt(18)\n\t"
    "v_smfmac_f32_16x16x64_bf16 v[104:107], a[212:215], v[76:83], v38 abid:1\n\t"
    "s_waitcnt vmcnt(17)\n\t"
    "v_smfmac_f32_16x16x64_bf16 v[108:111], a[216:219], v[76:83], v39\n\t"
    "s_waitcnt vmcnt(16)\n\t"
    "v_smfmac_f32_16x16x64_bf16 v[112:115], a[220:223], v[76:83], v39 abid:1\n\t"
    "ds_read_b128 v[76:79], v4 offset:3840\n\t"
    "s_waitcnt vmcnt(15)\n\t"
    "v_smfmac_f32_16x16x64_bf16 v[100:103], a[224:227], v[84:91], v40\n\t"
    "ds_read_b128 v[80:83], v5 offset:3840\n\t"
    "s_waitcnt vmcnt(14)\n\t"
    "v_smfmac_f32_16x16x64_bf16 v[104:107], a[228:231], v[84:91], v40 abid:1\n\t"
    "s_waitcnt vmcnt(13)\n\t"
    "v_smfmac_f32_16x16x64_bf16 v[108:111], a[232:235], v[84:91], v41\n\t"
    "s_waitcnt vmcnt(12)\n\t"
    "v_smfmac_f32_16x16x64_bf16 v[112:115], a[236:239], v[84:91], v41 abid:1\n\t"
    "ds_read_b128 v[84:87], v4 offset:5632\n\t"
    "s_waitcnt lgkmcnt(3)\n\t"
    "s_waitcnt vmcnt(11)\n\t"
    "v_smfmac_f32_16x16x64_bf16 v[100:103], a[240:243], v[92:99], v42\n\t"
    "ds_read_b128 v[88:91], v5 offset:5632\n\t"
    "s_waitcnt vmcnt(10)\n\t"
    "v_smfmac_f32_16x16x64_bf16 v[104:107], a[244:247], v[92:99], v42 abid:1\n\t"
    "s_waitcnt vmcnt(9)\n\t"
    "v_smfmac_f32_16x16x64_bf16 v[108:111], a[248:251], v[92:99], v43\n\t"
    "s_waitcnt vmcnt(8)\n\t"
    "v_smfmac_f32_16x16x64_bf16 v[112:115], a[252:255], v[92:99], v43 abid:1\n\t"
    "ds_read_b128 v[92:95], v2 offset:5888\n\t"
    "v_smfmac_f32_16x16x64_bf16 v[116:119], a[208:211], v[60:67], v38\n\t"
    "ds_read_b128 v[96:99], v3 offset:5888\n\t"
    "v_smfmac_f32_16x16x64_bf16 v[120:123], a[212:215], v[60:67], v38 abid:1\n\t"
    "v_smfmac_f32_16x16x64_bf16 v[124:127], a[216:219], v[60:67], v39\n\t"
    "v_cvt_pk_bf16_f32 v100, v100, v101\n\t"
    "v_smfmac_f32_16x16x64_bf16 v[128:131], a[220:223], v[60:67], v39 abid:1\n\t"
    "ds_read_b128 v[60:63], v4 offset:5888\n\t"
    "v_cvt_pk_bf16_f32 v101, v102, v103\n\t"
    "v_smfmac_f32_16x16x64_bf16 v[116:119], a[224:227], v[68:75], v40\n\t"
    "ds_read_b128 v[64:67], v5 offset:5888\n\t"
    "v_cvt_pk_bf16_f32 v102, v104, v105\n\t"
    "v_smfmac_f32_16x16x64_bf16 v[120:123], a[228:231], v[68:75], v40 abid:1\n\t"
    "v_cvt_pk_bf16_f32 v103, v106, v107\n\t"
    "v_smfmac_f32_16x16x64_bf16 v[124:127], a[232:235], v[68:75], v41\n\t"
    "v_cvt_pk_bf16_f32 v108, v108, v109\n\t"
    "v_smfmac_f32_16x16x64_bf16 v[128:131], a[236:239], v[68:75], v41 abid:1\n\t"
    "ds_read_b128 v[68:71], v4 offset:7168\n\t"
    "s_waitcnt lgkmcnt(3)\n\t"
    "v_cvt_pk_bf16_f32 v109, v110, v111\n\t"
    "v_smfmac_f32_16x16x64_bf16 v[116:119], a[240:243], v[76:83], v42\n\t"
    "ds_read_b128 v[72:75], v5 offset:7168\n\t"
    "v_cvt_pk_bf16_f32 v110, v112, v113\n\t"
    "v_smfmac_f32_16x16x64_bf16 v[120:123], a[244:247], v[76:83], v42 abid:1\n\t"
    "v_cvt_pk_bf16_f32 v111, v114, v115\n\t"
    "v_smfmac_f32_16x16x64_bf16 v[124:127], a[248:251], v[76:83], v43\n\t"
    "s_nop 0\n\t"
    "v_mov_b32_dpp v104, v108 row_ror:8 row_mask:0xf bank_mask:0xf\n\t"
    "v_smfmac_f32_16x16x64_bf16 v[128:131], a[252:255], v[76:83], v43 abid:1\n\t"
    "ds_read_b64 v[76:77], v228 offset:6144\n\t"
    "v_mov_b32_dpp v105, v109 row_ror:8 row_mask:0xf bank_mask:0xf\n\t"
    "v_smfmac_f32_16x16x64_bf16 v[236:239], a[208:211], v[84:91], v38\n\t"
    "ds_read_b64 v[78:79], v229 offset:6144\n\t"
    "v_mov_b32_dpp v106, v110 row_ror:8 row_mask:0xf bank_mask:0xf\n\t"
    "v_smfmac_f32_16x16x64_bf16 v[240:243], a[212:215], v[84:91], v38 abid:1\n\t"
    "ds_read_b64 v[80:81], v230 offset:6144\n\t"
    "v_mov_b32_dpp v107, v111 row_ror:8 row_mask:0xf bank_mask:0xf\n\t"
    "v_smfmac_f32_16x16x64_bf16 v[244:247], a[216:219], v[84:91], v39\n\t"
    "ds_read_b64 v[82:83], v231 offset:6144\n\t"
    "v_mov_b32_dpp v108, v100 row_ror:8 row_mask:0xf bank_mask:0x3\n\t"
    "v_smfmac_f32_16x16x64_bf16 v[248:251], a[220:223], v[84:91], v39 abid:1\n\t"
    "ds_read_b64 v[84:85], v232 offset:6144\n\t"
    "v_mov_b32_dpp v109, v101 row_ror:8 row_mask:0xf bank_mask:0x3\n\t"
    "v_smfmac_f32_16x16x64_bf16 v[236:239], a[224:227], v[92:99], v40\n\t"
    "ds_read_b64 v[86:87], v233 offset:6144\n\t"
    "v_mov_b32_dpp v110, v102 row_ror:8 row_mask:0xf bank_mask:0x3\n\t"
    "v_smfmac_f32_16x16x64_bf16 v[240:243], a[228:231], v[92:99], v40 abid:1\n\t"
    "ds_read_b64 v[88:89], v234 offset:6144\n\t"
    "v_mov_b32_dpp v111, v103 row_ror:8 row_mask:0xf bank_mask:0x3\n\t"
    "v_smfmac_f32_16x16x64_bf16 v[244:247], a[232:235], v[92:99], v41\n\t"
    "ds_read_b64 v[90:91], v235 offset:6144\n\t"
    "v_mov_b32_dpp v100, v104 quad_perm:[0,1,2,3] row_mask:0xf bank_mask:0xc\n\t"
    "v_smfmac_f32_16x16x64_bf16 v[248:251], a[236:239], v[92:99], v41 abid:1\n\t"
    "s_waitcnt lgkmcnt(0)\n\t"
    "s_barrier\n\t"
    "ds_read_b128 v[92:95], v2 offset:7424\n\t"
    "v_mov_b32_dpp v101, v105 quad_perm:[0,1,2,3] row_mask:0xf bank_mask:0xc\n\t"
    "v_smfmac_f32_16x16x64_bf16 v[236:239], a[240:243], v[60:67], v42\n\t"
    "s_add_u32 m0, s17, 0\n\t"
    "ds_read_b128 v[96:99], v3 offset:7424\n\t"
    "v_mov_b32_dpp v102, v106 quad_perm:[0,1,2,3] row_mask:0xf bank_mask:0xc\n\t"
    "v_smfmac_f32_16x16x64_bf16 v[240:243], a[244:247], v[60:67], v42 abid:1\n\t"
    "buffer_load_dwordx4 v6, s[8:11], s16 offen offset:0  lds\n\t"
    "v_mov_b32_dpp v103, v107 quad_perm:[0,1,2,3] row_mask:0xf bank_mask:0xc\n\t"
    "v_smfmac_f32_16x16x64_bf16 v[244:247], a[248:251], v[60:67], v43\n\t"
    "buffer_store_dwordx4 v[100:103], v0, s[48:51], s55 offen sc1\n\t"
    "buffer_store_dwordx4 v[108:111], v1, s[48:51], s55 offen sc1\n\t"
    "s_nop 1\n\t"
    "ds_read_b64 v[100:101], v200 offset:6144\n\t"
    "v_smfmac_f32_16x16x64_bf16 v[248:251], a[252:255], v[60:67], v43 abid:1\n\t"
    "ds_read_b64 v[60:61], v196 offset:6144\n\t"
    "s_add_u32 s54, s54, 32768\n\t"
    "s_xor_b32 s55, s54, s53\n\t"
    "v_cvt_pk_bf16_f32 v116, v116, v117\n\t"
    "v_cvt_pk_bf16_f32 v117, v118, v119\n\t"
    "v_cvt_pk_bf16_f32 v118, v120, v121\n\t"
    "v_cvt_pk_bf16_f32 v119, v122, v123\n\t"
    "v_cvt_pk_bf16_f32 v124, v124, v125\n\t"
    "v_cvt_pk_bf16_f32 v125, v126, v127\n\t"
    "v_cvt_pk_bf16_f32 v126, v128, v129\n\t"
    "v_smfmac_f32_16x16x64_bf16 v[44:47], a[144:147], v[68:75], v30\n\t"
    "buffer_load_dwordx4 v6, s[8:11], s16 offen offset:1024  lds\n\t"
    "ds_read_b64 v[62:63], v197 offset:6144\n\t"
    "v_cvt_pk_bf16_f32 v127, v130, v131\n\t"
    "v_smfmac_f32_16x16x64_bf16 v[48:51], a[148:151], v[68:75], v30 abid:1\n\t"
    "ds_read_b64 v[64:65], v198 offset:6144\n\t"
    "v_mov_b32_dpp v120, v124 row_ror:8 row_mask:0xf bank_mask:0xf\n\t"
    "v_smfmac_f32_16x16x64_bf16 v[52:55], a[152:155], v[68:75], v31\n\t"
    "ds_read_b64 v[66:67], v199 offset:6144\n\t"
    "s_add_u32 m0, s17, 10240\n\t"
    "v_mov_b32_dpp v121, v125 row_ror:8 row_mask:0xf bank_mask:0xf\n\t"
    "v_smfmac_f32_16x16x64_bf16 v[56:59], a[156:159], v[68:75], v31 abid:1\n\t"
    "ds_read_b64 v[68:69], v204 offset:6144\n\t"
    "buffer_load_dwordx4 v7, s[8:11], s16 offen offset:0  lds\n\t"
    "v_mov_b32_dpp v122, v126 row_ror:8 row_mask:0xf bank_mask:0xf\n\t"
    "v_smfmac_f32_16x16x64_bf16 v[44:47], v[180:183], v[76:83], v10\n\t"
    "ds_read_b64 v[70:71], v205 offset:6144\n\t"
    "v_mov_b32_dpp v123, v127 row_ror:8 row_mask:0xf bank_mask:0xf\n\t"
    "v_smfmac_f32_16x16x64_bf16 v[48:51], v[184:187], v[76:83], v10\n\t"
    "ds_read_b64 v[72:73], v206 offset:6144\n\t"
    "ds_read_b64 v[74:75], v207 offset:6144\n\t"
    "v_mov_b32_dpp v124, v116 row_ror:8 row_mask:0xf bank_mask:0x3\n\t"
    "v_smfmac_f32_16x16x64_bf16 v[52:55], v[188:191], v[84:91], v10\n\t"
    "ds_read_b128 v[76:79], v4 offset:7424\n\t"
    "buffer_load_dwordx4 v7, s[8:11], s16 offen offset:1024  lds\n\t"
    "v_mov_b32_dpp v125, v117 row_ror:8 row_mask:0xf bank_mask:0x3\n\t"
    "v_smfmac_f32_16x16x64_bf16 v[56:59], v[192:195], v[84:91], v10\n\t"
    "ds_read_b128 v[80:83], v5 offset:7424\n\t"
    "s_waitcnt lgkmcnt(8)\n\t"
    "v_mov_b32_dpp v126, v118 row_ror:8 row_mask:0xf bank_mask:0x3\n\t"
    "v_smfmac_f32_16x16x64_bf16 v[44:47], a[160:163], v[92:99], v32\n\t"
    "ds_read_b64 v[84:85], v212 offset:6144\n\t"
    "s_add_u32 m0, s17, 20480\n\t"
    "v_mov_b32_dpp v127, v119 row_ror:8 row_mask:0xf bank_mask:0x3\n\t"
    "v_smfmac_f32_16x16x64_bf16 v[48:51], a[164:167], v[92:99], v32 abid:1\n\t"
    "ds_read_b64 v[86:87], v213 offset:6144\n\t"
    "ds_read_b64 v[88:89], v214 offset:6144\n\t"
    "ds_read_b64 v[90:91], v215 offset:6144\n\t"
    "v_mov_b32_dpp v116, v120 quad_perm:[0,1,2,3] row_mask:0xf bank_mask:0xc\n\t"
    "v_smfmac_f32_16x16x64_bf16 v[52:55], a[168:171], v[92:99], v33\n\t"
    "buffer_load_dwordx4 v8, s[8:11], s16 offen offset:0  lds\n\t"
    "ds_read_b64 v[102:103], v201 offset:6144\n\t"
    "v_mov_b32_dpp v117, v121 quad_perm:[0,1,2,3] row_mask:0xf bank_mask:0xc\n\t"
    "v_smfmac_f32_16x16x64_bf16 v[56:59], a[172:175], v[92:99], v33 abid:1\n\t"
    "ds_read_b64 v[92:93], v220 offset:6144\n\t"
    "s_waitcnt lgkmcnt(10)\n\t"
    "v_mov_b32_dpp v118, v122 quad_perm:[0,1,2,3] row_mask:0xf bank_mask:0xc\n\t"
    "v_smfmac_f32_16x16x64_bf16 v[44:47], v[148:151], v[60:67], v10\n\t"
    "ds_read_b64 v[94:95], v221 offset:6144\n\t"
    "s_waitcnt lgkmcnt(9)\n\t"
    "v_mov_b32_dpp v119, v123 quad_perm:[0,1,2,3] row_mask:0xf bank_mask:0xc\n\t"
    "v_smfmac_f32_16x16x64_bf16 v[48:51], v[152:155], v[68:75], v10\n\t"
    "ds_read_b64 v[96:97], v222 offset:6144\n\t"
    "ds_read_b64 v[98:99], v223 offset:6144\n\t"
    "ds_read_b128 v[60:63], v2 offset:7680\n\t"
    "buffer_store_dwordx4 v[116:119], v0, s[48:51], s55 offen sc1\n\t"
    "buffer_load_dwordx4 v8, s[8:11], s16 offen offset:1024  lds\n\t"
    "s_waitcnt lgkmcnt(10)\n\t"
    "v_smfmac_f32_16x16x64_bf16 v[44:47], a[176:179], v[76:83], v34\n\t"
    "ds_read_b128 v[64:67], v3 offset:7680\n\t"
    "ds_read_b128 v[68:71], v4 offset:7680\n\t"
    "buffer_store_dwordx4 v[124:127], v1, s[48:51], s55 offen sc1\n\t"
    "v_smfmac_f32_16x16x64_bf16 v[48:51], a[180:183], v[76:83], v34 abid:1\n\t"
    "ds_read_b128 v[72:75], v5 offset:7680\n\t"
    "s_add_u32 m0, s17, 30720\n\t"
    "s_nop 0\n\t"
    "buffer_load_dwordx4 v9, s[8:11], s16 offen offset:0  lds\n\t"
    "v_smfmac_f32_16x16x64_bf16 v[52:55], a[184:187], v[76:83], v35\n\t"
    "ds_read_b64 v[104:105], v202 offset:6144\n\t"
    "s_add_u32 s54, s54, 32768\n\t"
    "s_xor_b32 s55, s54, s53\n\t"
    "v_cvt_pk_bf16_f32 v236, v236, v237\n\t"
    "v_cvt_pk_bf16_f32 v237, v238, v239\n\t"
    "v_cvt_pk_bf16_f32 v238, v240, v241\n\t"
    "v_cvt_pk_bf16_f32 v239, v242, v243\n\t"
    "v_cvt_pk_bf16_f32 v244, v244, v245\n\t"
    "v_cvt_pk_bf16_f32 v245, v246, v247\n\t"
    "v_cvt_pk_bf16_f32 v246, v248, v249\n\t"
    "v_cvt_pk_bf16_f32 v247, v250, v251\n\t"
    "s_nop 1\n\t"
    "v_mov_b32_dpp v240, v244 row_ror:8 row_mask:0xf bank_mask:0xf\n\t"
    "v_mov_b32_dpp v241, v245 row_ror:8 row_mask:0xf bank_mask:0xf\n\t"
    "v_mov_b32_dpp v242, v246 row_ror:8 row_mask:0xf bank_mask:0xf\n\t"
    "v_mov_b32_dpp v243, v247 row_ror:8 row_mask:0xf bank_mask:0xf\n\t"
    "v_smfmac_f32_16x16x64_bf16 v[56:59], a[188:191], v[76:83], v35 abid:1\n\t"
    "ds_read_b64 v[106:107], v203 offset:6144\n\t"
    "s_waitcnt lgkmcnt(10)\n\t"
    "v_mov_b32_dpp v244, v236 row_ror:8 row_mask:0xf bank_mask:0x3\n\t"
    "v_smfmac_f32_16x16x64_bf16 v[52:55], v[156:159], v[84:91], v10\n\t"
    "ds_read_b64 v[108:109], v208 offset:6144\n\t"
    "ds_read_b64 v[110:111], v209 offset:6144\n\t"
    "ds_read_b64 v[112:113], v210 offset:6144\n\t"
    "ds_read_b64 v[114:115], v211 offset:6144\n\t"
    "ds_read_b64 v[116:117], v216 offset:6144\n\t"
    "ds_read_b64 v[118:119], v217 offset:6144\n\t"
    "ds_read_b64 v[120:121], v218 offset:6144\n\t"
    "ds_read_b64 v[122:123], v219 offset:6144\n\t"
    "ds_read_b64 v[124:125], v224 offset:6144\n\t"
    "ds_read_b64 v[126:127], v225 offset:6144\n\t"
    "ds_read_b64 v[128:129], v226 offset:6144\n\t"
    "ds_read_b64 v[130:131], v227 offset:6144\n\t"
    "ds_read_b128 v[76:79], v2 offset:7936\n\t"
    "s_waitcnt lgkmcnt(15)\n\t"
    "v_mov_b32_dpp v245, v237 row_ror:8 row_mask:0xf bank_mask:0x3\n\t"
    "v_smfmac_f32_16x16x64_bf16 v[56:59], v[160:163], v[92:99], v10\n\t"
    "ds_read_b128 v[80:83], v3 offset:7936\n\t"
    "ds_read_b128 v[84:87], v4 offset:7936\n\t"
    "buffer_load_dwordx4 v9, s[8:11], s16 offen offset:1024  lds\n\t"
    "v_mov_b32_dpp v246, v238 row_ror:8 row_mask:0xf bank_mask:0x3\n\t"
    "v_smfmac_f32_16x16x64_bf16 v[44:47], a[192:195], v[60:67], v36\n\t"
    "ds_read_b128 v[88:91], v5 offset:7936\n\t"
    "s_add_u32 s52, s52, 32768\n\t"
    "s_xor_b32 s16, s52, s53\n\t"
    "v_mov_b32_dpp v247, v239 row_ror:8 row_mask:0xf bank_mask:0x3\n\t"
    "v_smfmac_f32_16x16x64_bf16 v[48:51], a[196:199], v[60:67], v36 abid:1\n\t"
    "v_mov_b32_dpp v236, v240 quad_perm:[0,1,2,3] row_mask:0xf bank_mask:0xc\n\t"
    "v_smfmac_f32_16x16x64_bf16 v[52:55], a[200:203], v[60:67], v37\n\t"
    "v_mov_b32_dpp v237, v241 quad_perm:[0,1,2,3] row_mask:0xf bank_mask:0xc\n\t"
    "v_smfmac_f32_16x16x64_bf16 v[56:59], a[204:207], v[60:67], v37 abid:1\n\t"
    "v_mov_b32_dpp v238, v242 quad_perm:[0,1,2,3] row_mask:0xf bank_mask:0xc\n\t"
    "v_mov_b32_dpp v239, v243 quad_perm:[0,1,2,3] row_mask:0xf bank_mask:0xc\n\t"
    "buffer_store_dwordx4 v[236:239], v0, s[48:51], s55 offen sc1\n\t"
    "buffer_store_dwordx4 v[244:247], v1, s[48:51], s55 offen sc1\n\t"
    "s_add_u32 s54, s54, 32768\n\t"
    "s_xor_b32 s55, s54, s53\n\t"
    "s_waitcnt vmcnt(14) lgkmcnt(0)\n\t"
    "s_barrier\n\t"
    "s_mov_b32 s18, 3\n\t"
    "s_branch .LmidB_%=\n\t"
    ".LloopB_%=:\n\t"
    "ds_read_b128 v[68:71], v2 offset:2048\n\t"
    "v_smfmac_f32_16x16x64_bf16 v[44:47], a[208:211], v[84:91], v38\n\t"
    "s_add_u32 m0, s17, 6144\n\t"
    "ds_read_b128 v[72:75], v3 offset:2048\n\t"
    "v_smfmac_f32_16x16x64_bf16 v[44:47], a[224:227], v[92:99], v40\n\t"
    "buffer_load_dwordx4 v6, s[8:11], s16 offen offset:0  lds\n\t"
    "ds_read_b128 v[76:79], v4 offset:2048\n\t"
    "v_smfmac_f32_16x16x64_bf16 v[44:47], a[240:243], v[60:67], v42\n\t"
    "ds_read_b128 v[80:83], v5 offset:2048\n\t"
    "v_smfmac_f32_16x16x64_bf16 v[44:47], v[164:167], v[100:107], v10\n\t"
    "ds_read_b64 v[100:101], v228 offset:2048\n\t"
    "v_smfmac_f32_16x16x64_bf16 v[48:51], a[212:215], v[84:91], v38 abid:1\n\t"
    "ds_read_b64 v[102:103], v229 offset:2048\n\t"
    "v_smfmac_f32_16x16x64_bf16 v[48:51], a[228:231], v[92:99], v40 abid:1\n\t"
    "ds_read_b64 v[104:105], v230 offset:2048\n\t"
    "v_smfmac_f32_16x16x64_bf16 v[48:51], a[244:247], v[60:67], v42 abid:1\n\t"
    "ds_read_b64 v[106:107], v231 offset:2048\n\t"
    "v_smfmac_f32_16x16x64_bf16 v[48:51], v[168:171], v[108:115], v10\n\t"
    "ds_read_b64 v[108:109], v204 offset:2048\n\t"
    "v_cvt_pk_bf16_f32 v236, v44, v45\n\t"
    "v_smfmac_f32_16x16x64_bf16 v[52:55], a[216:219], v[84:91], v39\n\t"
    "buffer_load_dwordx4 v6, s[8:11], s16 offen offset:1024  lds\n\t"
    "ds_read_b64 v[110:111], v205 offset:2048\n\t"
    "v_cvt_pk_bf16_f32 v237, v46, v47\n\t"
    "v_smfmac_f32_16x16x64_bf16 v[52:55], a[232:235], v[92:99], v41\n\t"
    "ds_read_b64 v[112:113], v206 offset:2048\n\t"
    "v_mov_b64 v[44:45], v[132:133]\n\t"
    "v_smfmac_f32_16x16x64_bf16 v[52:55], a[248:251], v[60:67], v43\n\t"
    "ds_read_b64 v[114:115], v207 offset:2048\n\t"
    "v_mov_b64 v[46:47], v[134:135]\n\t"
    "v_smfmac_f32_16x16x64_bf16 v[52:55], v[172:175], v[116:123], v10\n\t"
    "ds_read_b64 v[116:117], v232 offset:2048\n\t"
    "v_cvt_pk_bf16_f32 v238, v48, v49\n\t"
    "v_smfmac_f32_16x16x64_bf16 v[56:59], a[220:223], v[84:91], v39 abid:1\n\t"
    "ds_read_b128 v[84:87], v2 offset:2304\n\t"
    "v_cvt_pk_bf16_f32 v239, v50, v51\n\t"
    "v_smfmac_f32_16x16x64_bf16 v[56:59], a[236:239], v[92:99], v41 abid:1\n\t"
    "ds_read_b128 v[88:91], v3 offset:2304\n\t"
    "v_mov_b64 v[48:49], v[136:137]\n\t"
    "v_smfmac_f32_16x16x64_bf16 v[56:59], a[252:255], v[60:67], v43 abid:1\n\t"
    "s_add_u32 m0, s17, 16384\n\t"
    "ds_read_b64 v[118:119], v233 offset:2048\n\t"
    "v_mov_b64 v[50:51], v[138:139]\n\t"
    "v_smfmac_f32_16x16x64_bf16 v[56:59], v[176:179], v[124:131], v10\n\t"
    "buffer_load_dwordx4 v7, s[8:11], s16 offen offset:0  lds\n\t"
    "s_waitcnt lgkmcnt(6)\n\t"
    "v_cvt_pk_bf16_f32 v240, v52, v53\n\t"
    "v_smfmac_f32_16x16x64_bf16 v[44:47], a[0:3], v[68:75], v12\n\t"
    "ds_read_b64 v[120:121], v234 offset:2048\n\t"
    "v_cvt_pk_bf16_f32 v241, v54, v55\n\t"
    "v_smfmac_f32_16x16x64_bf16 v[48:51], a[4:7], v[68:75], v12 abid:1\n\t"
    "ds_read_b64 v[122:123], v235 offset:2048\n\t"
    "v_mov_b64 v[52:53], v[140:141]\n\t"
    "v_smfmac_f32_16x16x64_bf16 v[44:47], a[16:19], v[76:83], v14\n\t"
    "ds_read_b128 v[92:95], v4 offset:2304\n\t"
    "v_mov_b64 v[54:55], v[142:143]\n\t"
    "v_smfmac_f32_16x16x64_bf16 v[48:51], a[20:23], v[76:83], v14 abid:1\n\t"
    "ds_read_b128 v[96:99], v5 offset:2304\n\t"
    "s_waitcnt lgkmcnt(5)\n\t"
    "v_cvt_pk_bf16_f32 v242, v56, v57\n\t"
    "v_smfmac_f32_16x16x64_bf16 v[44:47], a[32:35], v[84:91], v16\n\t"
    "ds_read_b128 v[60:63], v2 offset:2560\n\t"
    "v_cvt_pk_bf16_f32 v243, v58, v59\n\t"
    "v_smfmac_f32_16x16x64_bf16 v[52:55], a[8:11], v[68:75], v13\n\t"
    "ds_read_b128 v[64:67], v3 offset:2560\n\t"
    "v_mov_b64 v[56:57], v[144:145]\n\t"
    "v_smfmac_f32_16x16x64_bf16 v[48:51], a[36:39], v[84:91], v16 abid:1\n\t"
    "buffer_load_dwordx4 v7, s[8:11], s16 offen offset:1024  lds\n\t"
    "ds_read_b64 v[124:125], v220 offset:2048\n\t"
    "v_mov_b64 v[58:59], v[146:147]\n\t"
    "v_smfmac_f32_16x16x64_bf16 v[52:55], a[24:27], v[76:83], v15\n\t"
    "ds_read_b64 v[126:127], v221 offset:2048\n\t"
    "v_mov_b32_dpp v244, v240 row_ror:8 row_mask:0xf bank_mask:0xf\n\t"
    "v_smfmac_f32_16x16x64_bf16 v[52:55], a[40:43], v[84:91], v17\n\t"
    "ds_read_b64 v[128:129], v222 offset:2048\n\t"
    "v_mov_b32_dpp v245, v241 row_ror:8 row_mask:0xf bank_mask:0xf\n\t"
    "v_smfmac_f32_16x16x64_bf16 v[56:59], a[12:15], v[68:75], v13 abid:1\n\t"
    "ds_read_b128 v[68:71], v4 offset:2560\n\t"
    "v_mov_b32_dpp v246, v242 row_ror:8 row_mask:0xf bank_mask:0xf\n\t"
    "v_smfmac_f32_16x16x64_bf16 v[56:59], a[28:31], v[76:83], v15 abid:1\n\t"
    "ds_read_b128 v[72:75], v5 offset:2560\n\t"
    "v_mov_b32_dpp v247, v243 row_ror:8 row_mask:0xf bank_mask:0xf\n\t"
    "v_smfmac_f32_16x16x64_bf16 v[56:59], a[44:47], v[84:91], v17 abid:1\n\t"
    "ds_read_b128 v[76:79], v2 offset:2816\n\t"
    "v_mov_b32_dpp v240, v236 row_ror:8 row_mask:0xf bank_mask:0x3\n\t"
    "v_smfmac_f32_16x16x64_bf16 v[44:47], v[180:183], v[100:107], v10\n\t"
    "s_add_u32 m0, s17, 26624\n\t"
    "ds_read_b128 v[80:83], v3 offset:2816\n\t"
    "v_mov_b32_dpp v241, v237 row_ror:8 row_mask:0xf bank_mask:0x3\n\t"
    "v_smfmac_f32_16x16x64_bf16 v[48:51], v[184:187], v[100:107], v10\n\t"
    "buffer_load_dwordx4 v8, s[8:11], s16 offen offset:0  lds\n\t"
    "s_waitcnt lgkmcnt(6)\n\t"
    "v_mov_b32_dpp v242, v238 row_ror:8 row_mask:0xf bank_mask:0x3\n\t"
    "v_smfmac_f32_16x16x64_bf16 v[52:55], v[188:191], v[116:123], v10\n\t"
    "ds_read_b64 v[100:101], v196 offset:2048\n\t"
    "v_mov_b32_dpp v243, v239 row_ror:8 row_mask:0xf bank_mask:0x3\n\t"
    "v_smfmac_f32_16x16x64_bf16 v[56:59], v[192:195], v[116:123], v10\n\t"
    "ds_read_b64 v[102:103], v197 offset:2048\n\t"
    "v_mov_b32_dpp v236, v244 quad_perm:[0,1,2,3] row_mask:0xf bank_mask:0xc\n\t"
    "v_smfmac_f32_16x16x64_bf16 v[44:47], a[48:51], v[92:99], v18\n\t"
    "ds_read_b64 v[104:105], v198 offset:2048\n\t"
    "v_mov_b32_dpp v237, v245 quad_perm:[0,1,2,3] row_mask:0xf bank_mask:0xc\n\t"
    "v_smfmac_f32_16x16x64_bf16 v[48:51], a[52:55], v[92:99], v18 abid:1\n\t"
    "ds_read_b64 v[106:107], v199 offset:2048\n\t"
    "v_mov_b32_dpp v238, v246 quad_perm:[0,1,2,3] row_mask:0xf bank_mask:0xc\n\t"
    "v_smfmac_f32_16x16x64_bf16 v[52:55], a[56:59], v[92:99], v19\n\t"
    "ds_read_b64 v[116:117], v212 offset:2048\n\t"
    "v_mov_b32_dpp v239, v247 quad_perm:[0,1,2,3] row_mask:0xf bank_mask:0xc\n\t"
    "v_smfmac_f32_16x16x64_bf16 v[56:59], a[60:63], v[92:99], v19 abid:1\n\t"
    "ds_read_b64 v[118:119], v213 offset:2048\n\t"
    "v_smfmac_f32_16x16x64_bf16 v[44:47], a[64:67], v[60:67], v20\n\t"
    "buffer_load_dwordx4 v8, s[8:11], s16 offen offset:1024  lds\n\t"
    "ds_read_b64 v[120:121], v214 offset:2048\n\t"
    "v_smfmac_f32_16x16x64_bf16 v[48:51], a[68:71], v[60:67], v20 abid:1\n\t"
    "ds_read_b64 v[122:123], v215 offset:2048\n\t"
    "buffer_store_dwordx4 v[236:239], v0, s[48:51], s55 offen sc1\n\t"
    "v_smfmac_f32_16x16x64_bf16 v[52:55], a[72:75], v[60:67], v21\n\t"
    "ds_read_b64 v[130:131], v223 offset:2048\n\t"
    "v_smfmac_f32_16x16x64_bf16 v[56:59], a[76:79], v[60:67], v21 abid:1\n\t"
    "ds_read_b128 v[84:87], v4 offset:2816\n\t"
    "s_waitcnt lgkmcnt(7)\n\t"
    "v_smfmac_f32_16x16x64_bf16 v[44:47], a[80:83], v[68:75], v22\n\t"
    "ds_read_b128 v[88:91], v5 offset:2816\n\t"
    "v_smfmac_f32_16x16x64_bf16 v[48:51], a[84:87], v[68:75], v22 abid:1\n\t"
    "ds_read_b128 v[92:95], v2 offset:3072\n\t"
    "v_smfmac_f32_16x16x64_bf16 v[52:55], a[88:91], v[68:75], v23\n\t"
    "s_add_u32 m0, s17, 36864\n\t"
    "ds_read_b128 v[96:99], v3 offset:3072\n\t"
    "v_smfmac_f32_16x16x64_bf16 v[56:59], a[92:95], v[68:75], v23 abid:1\n\t"
    "buffer_load_dwordx4 v9, s[8:11], s16 offen offset:0  lds\n\t"
    "buffer_store_dwordx4 v[240:243], v1, s[48:51], s55 offen sc1\n\t"
    "ds_read_b128 v[60:63], v4 offset:3072\n\t"
    "v_smfmac_f32_16x16x64_bf16 v[44:47], a[96:99], v[76:83], v24\n\t"
    "ds_read_b128 v[64:67], v5 offset:3072\n\t"
    "s_add_u32 s54, s54, 32768\n\t"
    "s_xor_b32 s55, s54, s53\n\t"
    "v_smfmac_f32_16x16x64_bf16 v[48:51], a[100:103], v[76:83], v24 abid:1\n\t"
    "ds_read_b128 v[68:71], v2 offset:3328\n\t"
    "v_smfmac_f32_16x16x64_bf16 v[52:55], a[104:107], v[76:83], v25\n\t"
    "ds_read_b128 v[72:75], v3 offset:3328\n\t"
    "v_smfmac_f32_16x16x64_bf16 v[56:59], a[108:111], v[76:83], v25 abid:1\n\t"
    "ds_read_b128 v[76:79], v4 offset:3328\n\t"
    "s_waitcnt lgkmcnt(7)\n\t"
    "v_smfmac_f32_16x16x64_bf16 v[44:47], v[148:151], v[100:107], v10\n\t"
    "ds_read_b64 v[100:101], v200 offset:2048\n\t"
    "v_smfmac_f32_16x16x64_bf16 v[48:51], v[152:155], v[108:115], v10\n\t"
    "ds_read_b64 v[102:103], v201 offset:2048\n\t"
    "v_smfmac_f32_16x16x64_bf16 v[52:55], v[156:159], v[116:123], v10\n\t"
    "buffer_load_dwordx4 v9, s[8:11], s16 offen offset:1024  lds\n\t"
    "s_add_u32 s52, s52, 32768\n\t"
    "s_xor_b32 s16, s52, s53\n\t"
    "ds_read_b64 v[104:105], v202 offset:2048\n\t"
    "v_smfmac_f32_16x16x64_bf16 v[56:59], v[160:163], v[124:131], v10\n\t"
    "ds_read_b64 v[106:107], v203 offset:2048\n\t"
    "v_smfmac_f32_16x16x64_bf16 v[44:47], a[112:115], v[84:91], v26\n\t"
    "ds_read_b128 v[80:83], v5 offset:3328\n\t"
    "v_smfmac_f32_16x16x64_bf16 v[48:51], a[116:119], v[84:91], v26 abid:1\n\t"
    "ds_read_b64 v[108:109], v208 offset:2048\n\t"
    "v_smfmac_f32_16x16x64_bf16 v[52:55], a[120:123], v[84:91], v27\n\t"
    "ds_read_b64 v[110:111], v209 offset:2048\n\t"
    "v_smfmac_f32_16x16x64_bf16 v[56:59], a[124:127], v[84:91], v27 abid:1\n\t"
    "ds_read_b64 v[112:113], v210 offset:2048\n\t"
    "s_waitcnt lgkmcnt(7)\n\t"
    "v_smfmac_f32_16x16x64_bf16 v[44:47], a[128:131], v[92:99], v28\n\t"
    "ds_read_b64 v[114:115], v211 offset:2048\n\t"
    "v_smfmac_f32_16x16x64_bf16 v[48:51], a[132:135], v[92:99], v28 abid:1\n\t"
    "ds_read_b128 v[84:87], v2 offset:3584\n\t"
    "v_smfmac_f32_16x16x64_bf16 v[52:55], a[136:139], v[92:99], v29\n\t"
    "ds_read_b128 v[88:91], v3 offset:3584\n\t"
    "v_smfmac_f32_16x16x64_bf16 v[56:59], a[140:143], v[92:99], v29 abid:1\n\t"
    "ds_read_b64 v[116:117], v216 offset:2048\n\t"
    "v_smfmac_f32_16x16x64_bf16 v[44:47], a[144:147], v[60:67], v30\n\t"
    "ds_read_b64 v[118:119], v217 offset:2048\n\t"
    "v_smfmac_f32_16x16x64_bf16 v[48:51], a[148:151], v[60:67], v30 abid:1\n\t"
    "ds_read_b64 v[120:121], v218 offset:2048\n\t"
    "v_smfmac_f32_16x16x64_bf16 v[52:55], a[152:155], v[60:67], v31\n\t"
    "ds_read_b64 v[122:123], v219 offset:2048\n\t"
    "v_smfmac_f32_16x16x64_bf16 v[56:59], a[156:159], v[60:67], v31 abid:1\n\t"
    "ds_read_b128 v[92:95], v4 offset:3584\n\t"
    "v_smfmac_f32_16x16x64_bf16 v[44:47], a[160:163], v[68:75], v32\n\t"
    "ds_read_b128 v[96:99], v5 offset:3584\n\t"
    "v_smfmac_f32_16x16x64_bf16 v[48:51], a[164:167], v[68:75], v32 abid:1\n\t"
    "ds_read_b128 v[60:63], v2 offset:3840\n\t"
    "v_smfmac_f32_16x16x64_bf16 v[52:55], a[168:171], v[68:75], v33\n\t"
    "ds_read_b128 v[64:67], v3 offset:3840\n\t"
    "v_smfmac_f32_16x16x64_bf16 v[56:59], a[172:175], v[68:75], v33 abid:1\n\t"
    "ds_read_b64 v[124:125], v224 offset:2048\n\t"
    "s_waitcnt lgkmcnt(7)\n\t"
    "v_smfmac_f32_16x16x64_bf16 v[44:47], a[176:179], v[76:83], v34\n\t"
    "ds_read_b128 v[68:71], v4 offset:3840\n\t"
    "v_smfmac_f32_16x16x64_bf16 v[48:51], a[180:183], v[76:83], v34 abid:1\n\t"
    "ds_read_b64 v[126:127], v225 offset:2048\n\t"
    "ds_read_b128 v[72:75], v5 offset:3840\n\t"
    "ds_read_b64 v[128:129], v226 offset:2048\n\t"
    "ds_read_b64 v[130:131], v227 offset:2048\n\t"
    "v_smfmac_f32_16x16x64_bf16 v[52:55], a[184:187], v[76:83], v35\n\t"
    "v_smfmac_f32_16x16x64_bf16 v[56:59], a[188:191], v[76:83], v35 abid:1\n\t"
    "v_smfmac_f32_16x16x64_bf16 v[44:47], a[192:195], v[84:91], v36\n\t"
    "v_smfmac_f32_16x16x64_bf16 v[48:51], a[196:199], v[84:91], v36 abid:1\n\t"
    "v_smfmac_f32_16x16x64_bf16 v[52:55], a[200:203], v[84:91], v37\n\t"
    "v_smfmac_f32_16x16x64_bf16 v[56:59], a[204:207], v[84:91], v37 abid:1\n\t"
    "s_waitcnt vmcnt(10) lgkmcnt(0)\n\t"
    "s_barrier\n\t"
    "ds_read_b128 v[76:79], v2 offset:4096\n\t"
    "v_smfmac_f32_16x16x64_bf16 v[44:47], a[208:211], v[92:99], v38\n\t"
    "s_add_u32 m0, s17, 8192\n\t"
    "ds_read_b128 v[80:83], v3 offset:4096\n\t"
    "v_smfmac_f32_16x16x64_bf16 v[44:47], a[224:227], v[60:67], v40\n\t"
    "buffer_load_dwordx4 v6, s[8:11], s16 offen offset:0  lds\n\t"
    "ds_read_b128 v[84:87], v4 offset:4096\n\t"
    "v_smfmac_f32_16x16x64_bf16 v[44:47], a[240:243], v[68:75], v42\n\t"
    "ds_read_b128 v[88:91], v5 offset:4096\n\t"
    "v_smfmac_f32_16x16x64_bf16 v[44:47], v[164:167], v[100:107], v10\n\t"
    "ds_read_b64 v[100:101], v228 offset:4096\n\t"
    "v_smfmac_f32_16x16x64_bf16 v[48:51], a[212:215], v[92:99], v38 abid:1\n\t"
    "ds_read_b64 v[102:103], v229 offset:4096\n\t"
    "v_smfmac_f32_16x16x64_bf16 v[48:51], a[228:231], v[60:67], v40 abid:1\n\t"
    "ds_read_b64 v[104:105], v230 offset:4096\n\t"
    "v_smfmac_f32_16x16x64_bf16 v[48:51], a[244:247], v[68:75], v42 abid:1\n\t"
    "ds_read_b64 v[106:107], v231 offset:4096\n\t"
    "v_smfmac_f32_16x16x64_bf16 v[48:51], v[168:171], v[108:115], v10\n\t"
    "ds_read_b64 v[108:109], v204 offset:4096\n\t"
    "v_cvt_pk_bf16_f32 v236, v44, v45\n\t"
    "v_smfmac_f32_16x16x64_bf16 v[52:55], a[216:219], v[92:99], v39\n\t"
    "buffer_load_dwordx4 v6, s[8:11], s16 offen offset:1024  lds\n\t"
    "ds_read_b64 v[110:111], v205 offset:4096\n\t"
    "v_cvt_pk_bf16_f32 v237, v46, v47\n\t"
    "v_smfmac_f32_16x16x64_bf16 v[52:55], a[232:235], v[60:67], v41\n\t"
    "ds_read_b64 v[112:113], v206 offset:4096\n\t"
    "v_mov_b64 v[44:45], v[132:133]\n\t"
    "v_smfmac_f32_16x16x64_bf16 v[52:55], a[248:251], v[68:75], v43\n\t"
    "ds_read_b64 v[114:115], v207 offset:4096\n\t"
    "v_mov_b64 v[46:47], v[134:135]\n\t"
    "v_smfmac_f32_16x16x64_bf16 v[52:55], v[172:175], v[116:123], v10\n\t"
    "ds_read_b64 v[116:117], v232 offset:4096\n\t"
    "v_cvt_pk_bf16_f32 v238, v48, v49\n\t"
    "v_smfmac_f32_16x16x64_bf16 v[56:59], a[220:223], v[92:99], v39 abid:1\n\t"
    "ds_read_b128 v[92:95], v2 offset:4352\n\t"
    "v_cvt_pk_bf16_f32 v239, v50, v51\n\t"
    "v_smfmac_f32_16x16x64_bf16 v[56:59], a[236:239], v[60:67], v41 abid:1\n\t"
    "ds_read_b128 v[96:99], v3 offset:4352\n\t"
    "v_mov_b64 v[48:49], v[136:137]\n\t"
    "v_smfmac_f32_16x16x64_bf16 v[56:59], a[252:255], v[68:75], v43 abid:1\n\t"
    "s_add_u32 m0, s17, 18432\n\t"
    "ds_read_b64 v[118:119], v233 offset:4096\n\t"
    "v_mov_b64 v[50:51], v[138:139]\n\t"
    "v_smfmac_f32_16x16x64_bf16 v[56:59], v[176:179], v[124:131], v10\n\t"
    "buffer_load_dwordx4 v7, s[8:11], s16 offen offset:0  lds\n\t"
    "s_waitcnt lgkmcnt(6)\n\t"
    "v_cvt_pk_bf16_f32 v240, v52, v53\n\t"
    "v_smfmac_f32_16x16x64_bf16 v[44:47], a[0:3], v[76:83], v12\n\t"
    "ds_read_b64 v[120:121], v234 offset:4096\n\t"
    "v_cvt_pk_bf16_f32 v241, v54, v55\n\t"
    "v_smfmac_f32_16x16x64_bf16 v[48:51], a[4:7], v[76:83], v12 abid:1\n\t"
    "ds_read_b64 v[122:123], v235 offset:4096\n\t"
    "v_mov_b64 v[52:53], v[140:141]\n\t"
    "v_smfmac_f32_16x16x64_bf16 v[44:47], a[16:19], v[84:91], v14\n\t"
    "ds_read_b128 v[60:63], v4 offset:4352\n\t"
    "v_mov_b64 v[54:55], v[142:143]\n\t"
    "v_smfmac_f32_16x16x64_bf16 v[48:51], a[20:23], v[84:91], v14 abid:1\n\t"
    "ds_read_b128 v[64:67], v5 offset:4352\n\t"
    "s_waitcnt lgkmcnt(5)\n\t"
    "v_cvt_pk_bf16_f32 v242, v56, v57\n\t"
    "v_smfmac_f32_16x16x64_bf16 v[44:47], a[32:35], v[92:99], v16\n\t"
    "ds_read_b128 v[68:71], v2 offset:4608\n\t"
    "v_cvt_pk_bf16_f32 v243, v58, v59\n\t"
    "v_smfmac_f32_16x16x64_bf16 v[52:55], a[8:11], v[76:83], v13\n\t"
    "ds_read_b128 v[72:75], v3 offset:4608\n\t"
    "v_mov_b64 v[56:57], v[144:145]\n\t"
    "v_smfmac_f32_16x16x64_bf16 v[48:51], a[36:39], v[92:99], v16 abid:1\n\t"
    "buffer_load_dwordx4 v7, s[8:11], s16 offen offset:1024  lds\n\t"
    "ds_read_b64 v[124:125], v220 offset:4096\n\t"
    "v_mov_b64 v[58:59], v[146:147]\n\t"
    "v_smfmac_f32_16x16x64_bf16 v[52:55], a[24:27], v[84:91], v15\n\t"
    "ds_read_b64 v[126:127], v221 offset:4096\n\t"
    "v_mov_b32_dpp v244, v240 row_ror:8 row_mask:0xf bank_mask:0xf\n\t"
    "v_smfmac_f32_16x16x64_bf16 v[52:55], a[40:43], v[92:99], v17\n\t"
    "ds_read_b64 v[128:129], v222 offset:4096\n\t"
    "v_mov_b32_dpp v245, v241 row_ror:8 row_mask:0xf bank_mask:0xf\n\t"
    "v_smfmac_f32_16x16x64_bf16 v[56:59], a[12:15], v[76:83], v13 abid:1\n\t"
    "ds_read_b128 v[76:79], v4 offset:4608\n\t"
    "v_mov_b32_dpp v246, v242 row_ror:8 row_mask:0xf bank_mask:0xf\n\t"
    "v_smfmac_f32_16x16x64_bf16 v[56:59], a[28:31], v[84:91], v15 abid:1\n\t"
    "ds_read_b128 v[80:83], v5 offset:4608\n\t"
    "v_mov_b32_dpp v247, v243 row_ror:8 row_mask:0xf bank_mask:0xf\n\t"
    "v_smfmac_f32_16x16x64_bf16 v[56:59], a[44:47], v[92:99], v17 abid:1\n\t"
    "ds_read_b128 v[84:87], v2 offset:4864\n\t"
    "v_mov_b32_dpp v240, v236 row_ror:8 row_mask:0xf bank_mask:0x3\n\t"
    "v_smfmac_f32_16x16x64_bf16 v[44:47], v[180:183], v[100:107], v10\n\t"
    "s_add_u32 m0, s17, 28672\n\t"
    "ds_read_b128 v[88:91], v3 offset:4864\n\t"
    "v_mov_b32_dpp v241, v237 row_ror:8 row_mask:0xf bank_mask:0x3\n\t"
    "v_smfmac_f32_16x16x64_bf16 v[48:51], v[184:187], v[100:107], v10\n\t"
    "buffer_load_dwordx4 v8, s[8:11], s16 offen offset:0  lds\n\t"
    "s_waitcnt lgkmcnt(6)\n\t"
    "v_mov_b32_dpp v242, v238 row_ror:8 row_mask:0xf bank_mask:0x3\n\t"
    "v_smfmac_f32_16x16x64_bf16 v[52:55], v[188:191], v[116:123], v10\n\t"
    "ds_read_b64 v[100:101], v196 offset:4096\n\t"
    "v_mov_b32_dpp v243, v239 row_ror:8 row_mask:0xf bank_mask:0x3\n\t"
    "v_smfmac_f32_16x16x64_bf16 v[56:59], v[192:195], v[116:123], v10\n\t"
    "ds_read_b64 v[102:103], v197 offset:4096\n\t"
    "v_mov_b32_dpp v236, v244 quad_perm:[0,1,2,3] row_mask:0xf bank_mask:0xc\n\t"
    "v_smfmac_f32_16x16x64_bf16 v[44:47], a[48:51], v[60:67], v18\n\t"
    "ds_read_b64 v[104:105], v198 offset:4096\n\t"
    "v_mov_b32_dpp v237, v245 quad_perm:[0,1,2,3] row_mask:0xf bank_mask:0xc\n\t"
    "v_smfmac_f32_16x16x64_bf16 v[48:51], a[52:55], v[60:67], v18 abid:1\n\t"
    "ds_read_b64 v[106:107], v199 offset:4096\n\t"
    "v_mov_b32_dpp v238, v246 quad_perm:[0,1,2,3] row_mask:0xf bank_mask:0xc\n\t"
    "v_smfmac_f32_16x16x64_bf16 v[52:55], a[56:59], v[60:67], v19\n\t"
    "ds_read_b64 v[116:117], v212 offset:4096\n\t"
    "v_mov_b32_dpp v239, v247 quad_perm:[0,1,2,3] row_mask:0xf bank_mask:0xc\n\t"
    "v_smfmac_f32_16x16x64_bf16 v[56:59], a[60:63], v[60:67], v19 abid:1\n\t"
    "ds_read_b64 v[118:119], v213 offset:4096\n\t"
    "v_smfmac_f32_16x16x64_bf16 v[44:47], a[64:67], v[68:75], v20\n\t"
    "buffer_load_dwordx4 v8, s[8:11], s16 offen offset:1024  lds\n\t"
    "ds_read_b64 v[120:121], v214 offset:4096\n\t"
    "v_smfmac_f32_16x16x64_bf16 v[48:51], a[68:71], v[68:75], v20 abid:1\n\t"
    "ds_read_b64 v[122:123], v215 offset:4096\n\t"
    "buffer_store_dwordx4 v[236:239], v0, s[48:51], s55 offen sc1\n\t"
    "v_smfmac_f32_16x16x64_bf16 v[52:55], a[72:75], v[68:75], v21\n\t"
    "ds_read_b64 v[130:131], v223 offset:4096\n\t"
    "v_smfmac_f32_16x16x64_bf16 v[56:59], a[76:79], v[68:75], v21 abid:1\n\t"
    "ds_read_b128 v[92:95], v4 offset:4864\n\t"
    "s_waitcnt lgkmcnt(7)\n\t"
    "v_smfmac_f32_16x16x64_bf16 v[44:47], a[80:83], v[76:83], v22\n\t"
    "ds_read_b128 v[96:99], v5 offset:4864\n\t"
    "v_smfmac_f32_16x16x64_bf16 v[48:51], a[84:87], v[76:83], v22 abid:1\n\t"
    "ds_read_b128 v[60:63], v2 offset:5120\n\t"
    "v_smfmac_f32_16x16x64_bf16 v[52:55], a[88:91], v[76:83], v23\n\t"
    "s_add_u32 m0, s17, 38912\n\t"
    "ds_read_b128 v[64:67], v3 offset:5120\n\t"
    "v_smfmac_f32_16x16x64_bf16 v[56:59], a[92:95], v[76:83], v23 abid:1\n\t"
    "buffer_load_dwordx4 v9, s[8:11], s16 offen offset:0  lds\n\t"
    "buffer_store_dwordx4 v[240:243], v1, s[48:51], s55 offen sc1\n\t"
    "ds_read_b128 v[68:71], v4 offset:5120\n\t"
    "v_smfmac_f32_16x16x64_bf16 v[44:47], a[96:99], v[84:91], v24\n\t"
    "ds_read_b128 v[72:75], v5 offset:5120\n\t"
    "s_add_u32 s54, s54, 32768\n\t"
    "s_xor_b32 s55, s54, s53\n\t"
    "v_smfmac_f32_16x16x64_bf16 v[48:51], a[100:103], v[84:91], v24 abid:1\n\t"
    "ds_read_b128 v[76:79], v2 offset:5376\n\t"
    "v_smfmac_f32_16x16x64_bf16 v[52:55], a[104:107], v[84:91], v25\n\t"
    "ds_read_b128 v[80:83], v3 offset:5376\n\t"
    "v_smfmac_f32_16x16x64_bf16 v[56:59], a[108:111], v[84:91], v25 abid:1\n\t"
    "ds_read_b128 v[84:87], v4 offset:5376\n\t"
    "s_waitcnt lgkmcnt(7)\n\t"
    "v_smfmac_f32_16x16x64_bf16 v[44:47], v[148:151], v[100:107], v10\n\t"
    "ds_read_b64 v[100:101], v200 offset:4096\n\t"
    "v_smfmac_f32_16x16x64_bf16 v[48:51], v[152:155], v[108:115], v10\n\t"
    "ds_read_b64 v[102:103], v201 offset:4096\n\t"
    "v_smfmac_f32_16x16x64_bf16 v[52:55], v[156:159], v[116:123], v10\n\t"
    "buffer_load_dwordx4 v9, s[8:11], s16 offen offset:1024  lds\n\t"
    "s_add_u32 s52, s52, 32768\n\t"
    "s_xor_b32 s16, s52, s53\n\t"
    "ds_read_b64 v[104:105], v202 offset:4096\n\t"
    "v_smfmac_f32_16x16x64_bf16 v[56:59], v[160:163], v[124:131], v10\n\t"
    "ds_read_b64 v[106:107], v203 offset:4096\n\t"
    "v_smfmac_f32_16x16x64_bf16 v[44:47], a[112:115], v[92:99], v26\n\t"
    "ds_read_b128 v[88:91], v5 offset:5376\n\t"
    "v_smfmac_f32_16x16x64_bf16 v[48:51], a[116:119], v[92:99], v26 abid:1\n\t"
    "ds_read_b64 v[108:109], v208 offset:4096\n\t"
    "v_smfmac_f32_16x16x64_bf16 v[52:55], a[120:123], v[92:99], v27\n\t"
    "ds_read_b64 v[110:111], v209 offset:4096\n\t"
    "v_smfmac_f32_16x16x64_bf16 v[56:59], a[124:127], v[92:99], v27 abid:1\n\t"
    "ds_read_b64 v[112:113], v210 offset:4096\n\t"
    "s_waitcnt lgkmcnt(7)\n\t"
    "v_smfmac_f32_16x16x64_bf16 v[44:47], a[128:131], v[60:67], v28\n\t"
    "ds_read_b64 v[114:115], v211 offset:4096\n\t"
    "v_smfmac_f32_16x16x64_bf16 v[48:51], a[132:135], v[60:67], v28 abid:1\n\t"
    "ds_read_b128 v[92:95], v2 offset:5632\n\t"
    "v_smfmac_f32_16x16x64_bf16 v[52:55], a[136:139], v[60:67], v29\n\t"
    "ds_read_b128 v[96:99], v3 offset:5632\n\t"
    "v_smfmac_f32_16x16x64_bf16 v[56:59], a[140:143], v[60:67], v29 abid:1\n\t"
    "ds_read_b64 v[116:117], v216 offset:4096\n\t"
    "v_smfmac_f32_16x16x64_bf16 v[44:47], a[144:147], v[68:75], v30\n\t"
    "ds_read_b64 v[118:119], v217 offset:4096\n\t"
    "v_smfmac_f32_16x16x64_bf16 v[48:51], a[148:151], v[68:75], v30 abid:1\n\t"
    "ds_read_b64 v[120:121], v218 offset:4096\n\t"
    "v_smfmac_f32_16x16x64_bf16 v[52:55], a[152:155], v[68:75], v31\n\t"
    "ds_read_b64 v[122:123], v219 offset:4096\n\t"
    "v_smfmac_f32_16x16x64_bf16 v[56:59], a[156:159], v[68:75], v31 abid:1\n\t"
    "ds_read_b128 v[60:63], v4 offset:5632\n\t"
    "v_smfmac_f32_16x16x64_bf16 v[44:47], a[160:163], v[76:83], v32\n\t"
    "ds_read_b128 v[64:67], v5 offset:5632\n\t"
    "v_smfmac_f32_16x16x64_bf16 v[48:51], a[164:167], v[76:83], v32 abid:1\n\t"
    "ds_read_b128 v[68:71], v2 offset:5888\n\t"
    "v_smfmac_f32_16x16x64_bf16 v[52:55], a[168:171], v[76:83], v33\n\t"
    "ds_read_b128 v[72:75], v3 offset:5888\n\t"
    "v_smfmac_f32_16x16x64_bf16 v[56:59], a[172:175], v[76:83], v33 abid:1\n\t"
    "ds_read_b64 v[124:125], v224 offset:4096\n\t"
    "s_waitcnt lgkmcnt(7)\n\t"
    "v_smfmac_f32_16x16x64_bf16 v[44:47], a[176:179], v[84:91], v34\n\t"
    "ds_read_b128 v[76:79], v4 offset:5888\n\t"
    "v_smfmac_f32_16x16x64_bf16 v[48:51], a[180:183], v[84:91], v34 abid:1\n\t"
    "ds_read_b64 v[126:127], v225 offset:4096\n\t"
    "ds_read_b128 v[80:83], v5 offset:5888\n\t"
    "ds_read_b64 v[128:129], v226 offset:4096\n\t"
    "ds_read_b64 v[130:131], v227 offset:4096\n\t"
    "v_smfmac_f32_16x16x64_bf16 v[52:55], a[184:187], v[84:91], v35\n\t"
    "v_smfmac_f32_16x16x64_bf16 v[56:59], a[188:191], v[84:91], v35 abid:1\n\t"
    "v_smfmac_f32_16x16x64_bf16 v[44:47], a[192:195], v[92:99], v36\n\t"
    "v_smfmac_f32_16x16x64_bf16 v[48:51], a[196:199], v[92:99], v36 abid:1\n\t"
    "v_smfmac_f32_16x16x64_bf16 v[52:55], a[200:203], v[92:99], v37\n\t"
    "v_smfmac_f32_16x16x64_bf16 v[56:59], a[204:207], v[92:99], v37 abid:1\n\t"
    "s_waitcnt vmcnt(10) lgkmcnt(0)\n\t"
    "s_barrier\n\t"
    "ds_read_b128 v[84:87], v2 offset:6144\n\t"
    "v_smfmac_f32_16x16x64_bf16 v[44:47], a[208:211], v[60:67], v38\n\t"
    "s_add_u32 m0, s17, 0\n\t"
    "ds_read_b128 v[88:91], v3 offset:6144\n\t"
    "v_smfmac_f32_16x16x64_bf16 v[44:47], a[224:227], v[68:75], v40\n\t"
    "buffer_load_dwordx4 v6, s[8:11], s16 offen offset:0  lds\n\t"
    "ds_read_b128 v[92:95], v4 offset:6144\n\t"
    "v_smfmac_f32_16x16x64_bf16 v[44:47], a[240:243], v[76:83], v42\n\t"
    "ds_read_b128 v[96:99], v5 offset:6144\n\t"
    "v_smfmac_f32_16x16x64_bf16 v[44:47], v[164:167], v[100:107], v10\n\t"
    "ds_read_b64 v[100:101], v228 offset:6144\n\t"
    "v_smfmac_f32_16x16x64_bf16 v[48:51], a[212:215], v[60:67], v38 abid:1\n\t"
    "ds_read_b64 v[102:103], v229 offset:6144\n\t"
    "v_smfmac_f32_16x16x64_bf16 v[48:51], a[228:231], v[68:75], v40 abid:1\n\t"
    "ds_read_b64 v[104:105], v230 offset:6144\n\t"
    "v_smfmac_f32_16x16x64_bf16 v[48:51], a[244:247], v[76:83], v42 abid:1\n\t"
    "ds_read_b64 v[106:107], v231 offset:6144\n\t"
    "v_smfmac_f32_16x16x64_bf16 v[48:51], v[168:171], v[108:115], v10\n\t"
    "ds_read_b64 v[108:109], v204 offset:6144\n\t"
    "v_cvt_pk_bf16_f32 v236, v44, v45\n\t"
    "v_smfmac_f32_16x16x64_bf16 v[52:55], a[216:219], v[60:67], v39\n\t"
    "buffer_load_dwordx4 v6, s[8:11], s16 offen offset:1024  lds\n\t"
    "ds_read_b64 v[110:111], v205 offset:6144\n\t"
    "v_cvt_pk_bf16_f32 v237, v46, v47\n\t"
    "v_smfmac_f32_16x16x64_bf16 v[52:55], a[232:235], v[68:75], v41\n\t"
    "ds_read_b64 v[112:113], v206 offset:6144\n\t"
    "v_mov_b64 v[44:45], v[132:133]\n\t"
    "v_smfmac_f32_16x16x64_bf16 v[52:55], a[248:251], v[76:83], v43\n\t"
    "ds_read_b64 v[114:115], v207 offset:6144\n\t"
    "v_mov_b64 v[46:47], v[134:135]\n\t"
    "v_smfmac_f32_16x16x64_bf16 v[52:55], v[172:175], v[116:123], v10\n\t"
    "ds_read_b64 v[116:117], v232 offset:6144\n\t"
    "v_cvt_pk_bf16_f32 v238, v48, v49\n\t"
    "v_smfmac_f32_16x16x64_bf16 v[56:59], a[220:223], v[60:67], v39 abid:1\n\t"
    "ds_read_b128 v[60:63], v2 offset:6400\n\t"
    "v_cvt_pk_bf16_f32 v239, v50, v51\n\t"
    "v_smfmac_f32_16x16x64_bf16 v[56:59], a[236:239], v[68:75], v41 abid:1\n\t"
    "ds_read_b128 v[64:67], v3 offset:6400\n\t"
    "v_mov_b64 v[48:49], v[136:137]\n\t"
    "v_smfmac_f32_16x16x64_bf16 v[56:59], a[252:255], v[76:83], v43 abid:1\n\t"
    "s_add_u32 m0, s17, 10240\n\t"
    "ds_read_b64 v[118:119], v233 offset:6144\n\t"
    "v_mov_b64 v[50:51], v[138:139]\n\t"
    "v_smfmac_f32_16x16x64_bf16 v[56:59], v[176:179], v[124:131], v10\n\t"
    "buffer_load_dwordx4 v7, s[8:11], s16 offen offset:0  lds\n\t"
    "s_waitcnt lgkmcnt(6)\n\t"
    "v_cvt_pk_bf16_f32 v240, v52, v53\n\t"
    "v_smfmac_f32_16x16x64_bf16 v[44:47], a[0:3], v[84:91], v12\n\t"
    "ds_read_b64 v[120:121], v234 offset:6144\n\t"
    "v_cvt_pk_bf16_f32 v241, v54, v55\n\t"
    "v_smfmac_f32_16x16x64_bf16 v[48:51], a[4:7], v[84:91], v12 abid:1\n\t"
    "ds_read_b64 v[122:123], v235 offset:6144\n\t"
    "v_mov_b64 v[52:53], v[140:141]\n\t"
    "v_smfmac_f32_16x16x64_bf16 v[44:47], a[16:19], v[92:99], v14\n\t"
    "ds_read_b128 v[68:71], v4 offset:6400\n\t"
    "v_mov_b64 v[54:55], v[142:143]\n\t"
    "v_smfmac_f32_16x16x64_bf16 v[48:51], a[20:23], v[92:99], v14 abid:1\n\t"
    "ds_read_b128 v[72:75], v5 offset:6400\n\t"
    "s_waitcnt lgkmcnt(5)\n\t"
    "v_cvt_pk_bf16_f32 v242, v56, v57\n\t"
    "v_smfmac_f32_16x16x64_bf16 v[44:47], a[32:35], v[60:67], v16\n\t"
    "ds_read_b128 v[76:79], v2 offset:6656\n\t"
    "v_cvt_pk_bf16_f32 v243, v58, v59\n\t"
    "v_smfmac_f32_16x16x64_bf16 v[52:55], a[8:11], v[84:91], v13\n\t"
    "ds_read_b128 v[80:83], v3 offset:6656\n\t"
    "v_mov_b64 v[56:57], v[144:145]\n\t"
    "v_smfmac_f32_16x16x64_bf16 v[48:51], a[36:39], v[60:67], v16 abid:1\n\t"
    "buffer_load_dwordx4 v7, s[8:11], s16 offen offset:1024  lds\n\t"
    "ds_read_b64 v[124:125], v220 offset:6144\n\t"
    "v_mov_b64 v[58:59], v[146:147]\n\t"
    "v_smfmac_f32_16x16x64_bf16 v[52:55], a[24:27], v[92:99], v15\n\t"
    "ds_read_b64 v[126:127], v221 offset:6144\n\t"
    "v_mov_b32_dpp v244, v240 row_ror:8 row_mask:0xf bank_mask:0xf\n\t"
    "v_smfmac_f32_16x16x64_bf16 v[52:55], a[40:43], v[60:67], v17\n\t"
    "ds_read_b64 v[128:129], v222 offset:6144\n\t"
    "v_mov_b32_dpp v245, v241 row_ror:8 row_mask:0xf bank_mask:0xf\n\t"
    "v_smfmac_f32_16x16x64_bf16 v[56:59], a[12:15], v[84:91], v13 abid:1\n\t"
    "ds_read_b128 v[84:87], v4 offset:6656\n\t"
    "v_mov_b32_dpp v246, v242 row_ror:8 row_mask:0xf bank_mask:0xf\n\t"
    "v_smfmac_f32_16x16x64_bf16 v[56:59], a[28:31], v[92:99], v15 abid:1\n\t"
    "ds_read_b128 v[88:91], v5 offset:6656\n\t"
    "v_mov_b32_dpp v247, v243 row_ror:8 row_mask:0xf bank_mask:0xf\n\t"
    "v_smfmac_f32_16x16x64_bf16 v[56:59], a[44:47], v[60:67], v17 abid:1\n\t"
    "ds_read_b128 v[92:95], v2 offset:6912\n\t"
    "v_mov_b32_dpp v240, v236 row_ror:8 row_mask:0xf bank_mask:0x3\n\t"
    "v_smfmac_f32_16x16x64_bf16 v[44:47], v[180:183], v[100:107], v10\n\t"
    "s_add_u32 m0, s17, 20480\n\t"
    "ds_read_b128 v[96:99], v3 offset:6912\n\t"
    "v_mov_b32_dpp v241, v237 row_ror:8 row_mask:0xf bank_mask:0x3\n\t"
    "v_smfmac_f32_16x16x64_bf16 v[48:51], v[184:187], v[100:107], v10\n\t"
    "buffer_load_dwordx4 v8, s[8:11], s16 offen offset:0  lds\n\t"
    "s_waitcnt lgkmcnt(6)\n\t"
    "v_mov_b32_dpp v242, v238 row_ror:8 row_mask:0xf bank_mask:0x3\n\t"
    "v_smfmac_f32_16x16x64_bf16 v[52:55], v[188:191], v[116:123], v10\n\t"
    "ds_read_b64 v[100:101], v196 offset:6144\n\t"
    "v_mov_b32_dpp v243, v239 row_ror:8 row_mask:0xf bank_mask:0x3\n\t"
    "v_smfmac_f32_16x16x64_bf16 v[56:59], v[192:195], v[116:123], v10\n\t"
    "ds_read_b64 v[102:103], v197 offset:6144\n\t"
    "v_mov_b32_dpp v236, v244 quad_perm:[0,1,2,3] row_mask:0xf bank_mask:0xc\n\t"
    "v_smfmac_f32_16x16x64_bf16 v[44:47], a[48:51], v[68:75], v18\n\t"
    "ds_read_b64 v[104:105], v198 offset:6144\n\t"
    "v_mov_b32_dpp v237, v245 quad_perm:[0,1,2,3] row_mask:0xf bank_mask:0xc\n\t"
    "v_smfmac_f32_16x16x64_bf16 v[48:51], a[52:55], v[68:75], v18 abid:1\n\t"
    "ds_read_b64 v[106:107], v199 offset:6144\n\t"
    "v_mov_b32_dpp v238, v246 quad_perm:[0,1,2,3] row_mask:0xf bank_mask:0xc\n\t"
    "v_smfmac_f32_16x16x64_bf16 v[52:55], a[56:59], v[68:75], v19\n\t"
    "ds_read_b64 v[116:117], v212 offset:6144\n\t"
    "v_mov_b32_dpp v239, v247 quad_perm:[0,1,2,3] row_mask:0xf bank_mask:0xc\n\t"
    "v_smfmac_f32_16x16x64_bf16 v[56:59], a[60:63], v[68:75], v19 abid:1\n\t"
    "ds_read_b64 v[118:119], v213 offset:6144\n\t"
    "v_smfmac_f32_16x16x64_bf16 v[44:47], a[64:67], v[76:83], v20\n\t"
    "buffer_load_dwordx4 v8, s[8:11], s16 offen offset:1024  lds\n\t"
    "ds_read_b64 v[120:121], v214 offset:6144\n\t"
    "v_smfmac_f32_16x16x64_bf16 v[48:51], a[68:71], v[76:83], v20 abid:1\n\t"
    "ds_read_b64 v[122:123], v215 offset:6144\n\t"
    "buffer_store_dwordx4 v[236:239], v0, s[48:51], s55 offen sc1\n\t"
    "v_smfmac_f32_16x16x64_bf16 v[52:55], a[72:75], v[76:83], v21\n\t"
    "ds_read_b64 v[130:131], v223 offset:6144\n\t"
    "v_smfmac_f32_16x16x64_bf16 v[56:59], a[76:79], v[76:83], v21 abid:1\n\t"
    "ds_read_b128 v[60:63], v4 offset:6912\n\t"
    "s_waitcnt lgkmcnt(7)\n\t"
    "v_smfmac_f32_16x16x64_bf16 v[44:47], a[80:83], v[84:91], v22\n\t"
    "ds_read_b128 v[64:67], v5 offset:6912\n\t"
    "v_smfmac_f32_16x16x64_bf16 v[48:51], a[84:87], v[84:91], v22 abid:1\n\t"
    "ds_read_b128 v[68:71], v2 offset:7168\n\t"
    "v_smfmac_f32_16x16x64_bf16 v[52:55], a[88:91], v[84:91], v23\n\t"
    "s_add_u32 m0, s17, 30720\n\t"
    "ds_read_b128 v[72:75], v3 offset:7168\n\t"
    "v_smfmac_f32_16x16x64_bf16 v[56:59], a[92:95], v[84:91], v23 abid:1\n\t"
    "buffer_load_dwordx4 v9, s[8:11], s16 offen offset:0  lds\n\t"
    "buffer_store_dwordx4 v[240:243], v1, s[48:51], s55 offen sc1\n\t"
    "ds_read_b128 v[76:79], v4 offset:7168\n\t"
    "v_smfmac_f32_16x16x64_bf16 v[44:47], a[96:99], v[92:99], v24\n\t"
    "ds_read_b128 v[80:83], v5 offset:7168\n\t"
    "s_add_u32 s54, s54, 32768\n\t"
    "s_xor_b32 s55, s54, s53\n\t"
    "v_smfmac_f32_16x16x64_bf16 v[48:51], a[100:103], v[92:99], v24 abid:1\n\t"
    "ds_read_b128 v[84:87], v2 offset:7424\n\t"
    "v_smfmac_f32_16x16x64_bf16 v[52:55], a[104:107], v[92:99], v25\n\t"
    "ds_read_b128 v[88:91], v3 offset:7424\n\t"
    "v_smfmac_f32_16x16x64_bf16 v[56:59], a[108:111], v[92:99], v25 abid:1\n\t"
    "ds_read_b128 v[92:95], v4 offset:7424\n\t"
    "s_waitcnt lgkmcnt(7)\n\t"
    "v_smfmac_f32_16x16x64_bf16 v[44:47], v[148:151], v[100:107], v10\n\t"
    "ds_read_b64 v[100:101], v200 offset:6144\n\t"
    "v_smfmac_f32_16x16x64_bf16 v[48:51], v[152:155], v[108:115], v10\n\t"
    "ds_read_b64 v[102:103], v201 offset:6144\n\t"
    "v_smfmac_f32_16x16x64_bf16 v[52:55], v[156:159], v[116:123], v10\n\t"
    "buffer_load_dwordx4 v9, s[8:11], s16 offen offset:1024  lds\n\t"
    "s_add_u32 s52, s52, 32768\n\t"
    "s_xor_b32 s16, s52, s53\n\t"
    "ds_read_b64 v[104:105], v202 offset:6144\n\t"
    "v_smfmac_f32_16x16x64_bf16 v[56:59], v[160:163], v[124:131], v10\n\t"
    "ds_read_b64 v[106:107], v203 offset:6144\n\t"
    "v_smfmac_f32_16x16x64_bf16 v[44:47], a[112:115], v[60:67], v26\n\t"
    "ds_read_b128 v[96:99], v5 offset:7424\n\t"
    "v_smfmac_f32_16x16x64_bf16 v[48:51], a[116:119], v[60:67], v26 abid:1\n\t"
    "ds_read_b64 v[108:109], v208 offset:6144\n\t"
    "v_smfmac_f32_16x16x64_bf16 v[52:55], a[120:123], v[60:67], v27\n\t"
    "ds_read_b64 v[110:111], v209 offset:6144\n\t"
    "v_smfmac_f32_16x16x64_bf16 v[56:59], a[124:127], v[60:67], v27 abid:1\n\t"
    "ds_read_b64 v[112:113], v210 offset:6144\n\t"
    "s_waitcnt lgkmcnt(7)\n\t"
    "v_smfmac_f32_16x16x64_bf16 v[44:47], a[128:131], v[68:75], v28\n\t"
    "ds_read_b64 v[114:115], v211 offset:6144\n\t"
    "v_smfmac_f32_16x16x64_bf16 v[48:51], a[132:135], v[68:75], v28 abid:1\n\t"
    "ds_read_b128 v[60:63], v2 offset:7680\n\t"
    "v_smfmac_f32_16x16x64_bf16 v[52:55], a[136:139], v[68:75], v29\n\t"
    "ds_read_b128 v[64:67], v3 offset:7680\n\t"
    "v_smfmac_f32_16x16x64_bf16 v[56:59], a[140:143], v[68:75], v29 abid:1\n\t"
    "ds_read_b64 v[116:117], v216 offset:6144\n\t"
    "v_smfmac_f32_16x16x64_bf16 v[44:47], a[144:147], v[76:83], v30\n\t"
    "ds_read_b64 v[118:119], v217 offset:6144\n\t"
    "v_smfmac_f32_16x16x64_bf16 v[48:51], a[148:151], v[76:83], v30 abid:1\n\t"
    "ds_read_b64 v[120:121], v218 offset:6144\n\t"
    "v_smfmac_f32_16x16x64_bf16 v[52:55], a[152:155], v[76:83], v31\n\t"
    "ds_read_b64 v[122:123], v219 offset:6144\n\t"
    "v_smfmac_f32_16x16x64_bf16 v[56:59], a[156:159], v[76:83], v31 abid:1\n\t"
    "ds_read_b128 v[68:71], v4 offset:7680\n\t"
    "v_smfmac_f32_16x16x64_bf16 v[44:47], a[160:163], v[84:91], v32\n\t"
    "ds_read_b128 v[72:75], v5 offset:7680\n\t"
    "v_smfmac_f32_16x16x64_bf16 v[48:51], a[164:167], v[84:91], v32 abid:1\n\t"
    "ds_read_b128 v[76:79], v2 offset:7936\n\t"
    "v_smfmac_f32_16x16x64_bf16 v[52:55], a[168:171], v[84:91], v33\n\t"
    "ds_read_b128 v[80:83], v3 offset:7936\n\t"
    "v_smfmac_f32_16x16x64_bf16 v[56:59], a[172:175], v[84:91], v33 abid:1\n\t"
    "ds_read_b64 v[124:125], v224 offset:6144\n\t"
    "s_waitcnt lgkmcnt(7)\n\t"
    "v_smfmac_f32_16x16x64_bf16 v[44:47], a[176:179], v[92:99], v34\n\t"
    "ds_read_b128 v[84:87], v4 offset:7936\n\t"
    "v_smfmac_f32_16x16x64_bf16 v[48:51], a[180:183], v[92:99], v34 abid:1\n\t"
    "ds_read_b64 v[126:127], v225 offset:6144\n\t"
    "ds_read_b128 v[88:91], v5 offset:7936\n\t"
    "ds_read_b64 v[128:129], v226 offset:6144\n\t"
    "ds_read_b64 v[130:131], v227 offset:6144\n\t"
    "v_smfmac_f32_16x16x64_bf16 v[52:55], a[184:187], v[92:99], v35\n\t"
    "v_smfmac_f32_16x16x64_bf16 v[56:59], a[188:191], v[92:99], v35 abid:1\n\t"
    "v_smfmac_f32_16x16x64_bf16 v[44:47], a[192:195], v[60:67], v36\n\t"
    "v_smfmac_f32_16x16x64_bf16 v[48:51], a[196:199], v[60:67], v36 abid:1\n\t"
    "v_smfmac_f32_16x16x64_bf16 v[52:55], a[200:203], v[60:67], v37\n\t"
    "v_smfmac_f32_16x16x64_bf16 v[56:59], a[204:207], v[60:67], v37 abid:1\n\t"
    "s_waitcnt vmcnt(10) lgkmcnt(0)\n\t"
    "s_barrier\n\t"
    ".LmidB_%=:\n\t"
    "ds_read_b128 v[92:95], v2 offset:8192\n\t"
    "v_smfmac_f32_16x16x64_bf16 v[44:47], a[208:211], v[68:75], v38\n\t"
    "s_add_u32 m0, s17, 2048\n\t"
    "ds_read_b128 v[96:99], v3 offset:8192\n\t"
    "v_smfmac_f32_16x16x64_bf16 v[44:47], a[224:227], v[76:83], v40\n\t"
    "buffer_load_dwordx4 v6, s[8:11], s16 offen offset:0  lds\n\t"
    "ds_read_b128 v[60:63], v4 offset:8192\n\t"
    "v_smfmac_f32_16x16x64_bf16 v[44:47], a[240:243], v[84:91], v42\n\t"
    "ds_read_b128 v[64:67], v5 offset:8192\n\t"
    "v_smfmac_f32_16x16x64_bf16 v[44:47], v[164:167], v[100:107], v10\n\t"
    "ds_read_b64 v[100:101], v228 offset:8192\n\t"
    "v_smfmac_f32_16x16x64_bf16 v[48:51], a[212:215], v[68:75], v38 abid:1\n\t"
    "ds_read_b64 v[102:103], v229 offset:8192\n\t"
    "v_smfmac_f32_16x16x64_bf16 v[48:51], a[228:231], v[76:83], v40 abid:1\n\t"
    "ds_read_b64 v[104:105], v230 offset:8192\n\t"
    "v_smfmac_f32_16x16x64_bf16 v[48:51], a[244:247], v[84:91], v42 abid:1\n\t"
    "ds_read_b64 v[106:107], v231 offset:8192\n\t"
    "v_smfmac_f32_16x16x64_bf16 v[48:51], v[168:171], v[108:115], v10\n\t"
    "ds_read_b64 v[108:109], v204 offset:8192\n\t"
    "v_cvt_pk_bf16_f32 v236, v44, v45\n\t"
    "v_smfmac_f32_16x16x64_bf16 v[52:55], a[216:219], v[68:75], v39\n\t"
    "buffer_load_dwordx4 v6, s[8:11], s16 offen offset:1024  lds\n\t"
    "ds_read_b64 v[110:111], v205 offset:8192\n\t"
    "v_cvt_pk_bf16_f32 v237, v46, v47\n\t"
    "v_smfmac_f32_16x16x64_bf16 v[52:55], a[232:235], v[76:83], v41\n\t"
    "ds_read_b64 v[112:113], v206 offset:8192\n\t"
    "v_mov_b64 v[44:45], v[132:133]\n\t"
    "v_smfmac_f32_16x16x64_bf16 v[52:55], a[248:251], v[84:91], v43\n\t"
    "ds_read_b64 v[114:115], v207 offset:8192\n\t"
    "v_mov_b64 v[46:47], v[134:135]\n\t"
    "v_smfmac_f32_16x16x64_bf16 v[52:55], v[172:175], v[116:123], v10\n\t"
    "ds_read_b64 v[116:117], v232 offset:8192\n\t"
    "v_cvt_pk_bf16_f32 v238, v48, v49\n\t"
    "v_smfmac_f32_16x16x64_bf16 v[56:59], a[220:223], v[68:75], v39 abid:1\n\t"
    "ds_read_b128 v[68:71], v2 offset:8448\n\t"
    "v_cvt_pk_bf16_f32 v239, v50, v51\n\t"
    "v_smfmac_f32_16x16x64_bf16 v[56:59], a[236:239], v[76:83], v41 abid:1\n\t"
    "ds_read_b128 v[72:75], v3 offset:8448\n\t"
    "v_mov_b64 v[48:49], v[136:137]\n\t"
    "v_smfmac_f32_16x16x64_bf16 v[56:59], a[252:255], v[84:91], v43 abid:1\n\t"
    "s_add_u32 m0, s17, 12288\n\t"
    "ds_read_b64 v[118:119], v233 offset:8192\n\t"
    "v_mov_b64 v[50:51], v[138:139]\n\t"
    "v_smfmac_f32_16x16x64_bf16 v[56:59], v[176:179], v[124:131], v10\n\t"
    "buffer_load_dwordx4 v7, s[8:11], s16 offen offset:0  lds\n\t"
    "s_waitcnt lgkmcnt(6)\n\t"
    "v_cvt_pk_bf16_f32 v240, v52, v53\n\t"
    "v_smfmac_f32_16x16x64_bf16 v[44:47], a[0:3], v[92:99], v12\n\t"
    "ds_read_b64 v[120:121], v234 offset:8192\n\t"
    "v_cvt_pk_bf16_f32 v241, v54, v55\n\t"
    "v_smfmac_f32_16x16x64_bf16 v[48:51], a[4:7], v[92:99], v12 abid:1\n\t"
    "ds_read_b64 v[122:123], v235 offset:8192\n\t"
    "v_mov_b64 v[52:53], v[140:141]\n\t"
    "v_smfmac_f32_16x16x64_bf16 v[44:47], a[16:19], v[60:67], v14\n\t"
    "ds_read_b128 v[76:79], v4 offset:8448\n\t"
    "v_mov_b64 v[54:55], v[142:143]\n\t"
    "v_smfmac_f32_16x16x64_bf16 v[48:51], a[20:23], v[60:67], v14 abid:1\n\t"
    "ds_read_b128 v[80:83], v5 offset:8448\n\t"
    "s_waitcnt lgkmcnt(5)\n\t"
    "v_cvt_pk_bf16_f32 v242, v56, v57\n\t"
    "v_smfmac_f32_16x16x64_bf16 v[44:47], a[32:35], v[68:75], v16\n\t"
    "ds_read_b128 v[84:87], v2 offset:8704\n\t"
    "v_cvt_pk_bf16_f32 v243, v58, v59\n\t"
    "v_smfmac_f32_16x16x64_bf16 v[52:55], a[8:11], v[92:99], v13\n\t"
    "ds_read_b128 v[88:91], v3 offset:8704\n\t"
    "v_mov_b64 v[56:57], v[144:145]\n\t"
    "v_smfmac_f32_16x16x64_bf16 v[48:51], a[36:39], v[68:75], v16 abid:1\n\t"
    "buffer_load_dwordx4 v7, s[8:11], s16 offen offset:1024  lds\n\t"
    "ds_read_b64 v[124:125], v220 offset:8192\n\t"
    "v_mov_b64 v[58:59], v[146:147]\n\t"
    "v_smfmac_f32_16x16x64_bf16 v[52:55], a[24:27], v[60:67], v15\n\t"
    "ds_read_b64 v[126:127], v221 offset:8192\n\t"
    "v_mov_b32_dpp v244, v240 row_ror:8 row_mask:0xf bank_mask:0xf\n\t"
    "v_smfmac_f32_16x16x64_bf16 v[52:55], a[40:43], v[68:75], v17\n\t"
    "ds_read_b64 v[128:129], v222 offset:8192\n\t"
    "v_mov_b32_dpp v245, v241 row_ror:8 row_mask:0xf bank_mask:0xf\n\t"
    "v_smfmac_f32_16x16x64_bf16 v[56:59], a[12:15], v[92:99], v13 abid:1\n\t"
    "ds_read_b128 v[92:95], v4 offset:8704\n\t"
    "v_mov_b32_dpp v246, v242 row_ror:8 row_mask:0xf bank_mask:0xf\n\t"
    "v_smfmac_f32_16x16x64_bf16 v[56:59], a[28:31], v[60:67], v15 abid:1\n\t"
    "ds_read_b128 v[96:99], v5 offset:8704\n\t"
    "v_mov_b32_dpp v247, v243 row_ror:8 row_mask:0xf bank_mask:0xf\n\t"
    "v_smfmac_f32_16x16x64_bf16 v[56:59], a[44:47], v[68:75], v17 abid:1\n\t"
    "ds_read_b128 v[60:63], v2 offset:8960\n\t"
    "v_mov_b32_dpp v240, v236 row_ror:8 row_mask:0xf bank_mask:0x3\n\t"
    "v_smfmac_f32_16x16x64_bf16 v[44:47], v[180:183], v[100:107], v10\n\t"
    "s_add_u32 m0, s17, 22528\n\t"
    "ds_read_b128 v[64:67], v3 offset:8960\n\t"
    "v_mov_b32_dpp v241, v237 row_ror:8 row_mask:0xf bank_mask:0x3\n\t"
    "v_smfmac_f32_16x16x64_bf16 v[48:51], v[184:187], v[100:107], v10\n\t"
    "buffer_load_dwordx4 v8, s[8:11], s16 offen offset:0  lds\n\t"
    "s_waitcnt lgkmcnt(6)\n\t"
    "v_mov_b32_dpp v242, v238 row_ror:8 row_mask:0xf bank_mask:0x3\n\t"
    "v_smfmac_f32_16x16x64_bf16 v[52:55], v[188:191], v[116:123], v10\n\t"
    "ds_read_b64 v[100:101], v196 offset:8192\n\t"
    "v_mov_b32_dpp v243, v239 row_ror:8 row_mask:0xf bank_mask:0x3\n\t"
    "v_smfmac_f32_16x16x64_bf16 v[56:59], v[192:195], v[116:123], v10\n\t"
    "ds_read_b64 v[102:103], v197 offset:8192\n\t"
    "v_mov_b32_dpp v236, v244 quad_perm:[0,1,2,3] row_mask:0xf bank_mask:0xc\n\t"
    "v_smfmac_f32_16x16x64_bf16 v[44:47], a[48:51], v[76:83], v18\n\t"
    "ds_read_b64 v[104:105], v198 offset:8192\n\t"
    "v_mov_b32_dpp v237, v245 quad_perm:[0,1,2,3] row_mask:0xf bank_mask:0xc\n\t"
    "v_smfmac_f32_16x16x64_bf16 v[48:51], a[52:55], v[76:83], v18 abid:1\n\t"
    "ds_read_b64 v[106:107], v199 offset:8192\n\t"
    "v_mov_b32_dpp v238, v246 quad_perm:[0,1,2,3] row_mask:0xf bank_mask:0xc\n\t"
    "v_smfmac_f32_16x16x64_bf16 v[52:55], a[56:59], v[76:83], v19\n\t"
    "ds_read_b64 v[116:117], v212 offset:8192\n\t"
    "v_mov_b32_dpp v239, v247 quad_perm:[0,1,2,3] row_mask:0xf bank_mask:0xc\n\t"
    "v_smfmac_f32_16x16x64_bf16 v[56:59], a[60:63], v[76:83], v19 abid:1\n\t"
    "ds_read_b64 v[118:119], v213 offset:8192\n\t"
    "v_smfmac_f32_16x16x64_bf16 v[44:47], a[64:67], v[84:91], v20\n\t"
    "buffer_load_dwordx4 v8, s[8:11], s16 offen offset:1024  lds\n\t"
    "ds_read_b64 v[120:121], v214 offset:8192\n\t"
    "v_smfmac_f32_16x16x64_bf16 v[48:51], a[68:71], v[84:91], v20 abid:1\n\t"
    "ds_read_b64 v[122:123], v215 offset:8192\n\t"
    "buffer_store_dwordx4 v[236:239], v0, s[48:51], s55 offen sc1\n\t"
    "v_smfmac_f32_16x16x64_bf16 v[52:55], a[72:75], v[84:91], v21\n\t"
    "ds_read_b64 v[130:131], v223 offset:8192\n\t"
    "v_smfmac_f32_16x16x64_bf16 v[56:59], a[76:79], v[84:91], v21 abid:1\n\t"
    "ds_read_b128 v[68:71], v4 offset:8960\n\t"
    "s_waitcnt lgkmcnt(7)\n\t"
    "v_smfmac_f32_16x16x64_bf16 v[44:47], a[80:83], v[92:99], v22\n\t"
    "ds_read_b128 v[72:75], v5 offset:8960\n\t"
    "v_smfmac_f32_16x16x64_bf16 v[48:51], a[84:87], v[92:99], v22 abid:1\n\t"
    "ds_read_b128 v[76:79], v2 offset:9216\n\t"
    "v_smfmac_f32_16x16x64_bf16 v[52:55], a[88:91], v[92:99], v23\n\t"
    "s_add_u32 m0, s17, 32768\n\t"
    "ds_read_b128 v[80:83], v3 offset:9216\n\t"
    "v_smfmac_f32_16x16x64_bf16 v[56:59], a[92:95], v[92:99], v23 abid:1\n\t"
    "buffer_load_dwordx4 v9, s[8:11], s16 offen offset:0  lds\n\t"
    "buffer_store_dwordx4 v[240:243], v1, s[48:51], s55 offen sc1\n\t"
    "ds_read_b128 v[84:87], v4 offset:9216\n\t"
    "v_smfmac_f32_16x16x64_bf16 v[44:47], a[96:99], v[60:67], v24\n\t"
    "ds_read_b128 v[88:91], v5 offset:9216\n\t"
    "s_add_u32 s54, s54, 32768\n\t"
    "s_xor_b32 s55, s54, s53\n\t"
    "v_smfmac_f32_16x16x64_bf16 v[48:51], a[100:103], v[60:67], v24 abid:1\n\t"
    "ds_read_b128 v[92:95], v2 offset:9472\n\t"
    "v_smfmac_f32_16x16x64_bf16 v[52:55], a[104:107], v[60:67], v25\n\t"
    "ds_read_b128 v[96:99], v3 offset:9472\n\t"
    "v_smfmac_f32_16x16x64_bf16 v[56:59], a[108:111], v[60:67], v25 abid:1\n\t"
    "ds_read_b128 v[60:63], v4 offset:9472\n\t"
    "s_waitcnt lgkmcnt(7)\n\t"
    "v_smfmac_f32_16x16x64_bf16 v[44:47], v[148:151], v[100:107], v10\n\t"
    "ds_read_b64 v[100:101], v200 offset:8192\n\t"
    "v_smfmac_f32_16x16x64_bf16 v[48:51], v[152:155], v[108:115], v10\n\t"
    "ds_read_b64 v[102:103], v201 offset:8192\n\t"
    "v_smfmac_f32_16x16x64_bf16 v[52:55], v[156:159], v[116:123], v10\n\t"
    "buffer_load_dwordx4 v9, s[8:11], s16 offen offset:1024  lds\n\t"
    "s_add_u32 s52, s52, 32768\n\t"
    "s_xor_b32 s16, s52, s53\n\t"
    "ds_read_b64 v[104:105], v202 offset:8192\n\t"
    "v_smfmac_f32_16x16x64_bf16 v[56:59], v[160:163], v[124:131], v10\n\t"
    "ds_read_b64 v[106:107], v203 offset:8192\n\t"
    "v_smfmac_f32_16x16x64_bf16 v[44:47], a[112:115], v[68:75], v26\n\t"
    "ds_read_b128 v[64:67], v5 offset:9472\n\t"
    "v_smfmac_f32_16x16x64_bf16 v[48:51], a[116:119], v[68:75], v26 abid:1\n\t"
    "ds_read_b64 v[108:109], v208 offset:8192\n\t"
    "v_smfmac_f32_16x16x64_bf16 v[52:55], a[120:123], v[68:75], v27\n\t"
    "ds_read_b64 v[110:111], v209 offset:8192\n\t"
    "v_smfmac_f32_16x16x64_bf16 v[56:59], a[124:127], v[68:75], v27 abid:1\n\t"
    "ds_read_b64 v[112:113], v210 offset:8192\n\t"
    "s_waitcnt lgkmcnt(7)\n\t"
    "v_smfmac_f32_16x16x64_bf16 v[44:47], a[128:131], v[76:83], v28\n\t"
    "ds_read_b64 v[114:115], v211 offset:8192\n\t"
    "v_smfmac_f32_16x16x64_bf16 v[48:51], a[132:135], v[76:83], v28 abid:1\n\t"
    "ds_read_b128 v[68:71], v2 offset:9728\n\t"
    "v_smfmac_f32_16x16x64_bf16 v[52:55], a[136:139], v[76:83], v29\n\t"
    "ds_read_b128 v[72:75], v3 offset:9728\n\t"
    "v_smfmac_f32_16x16x64_bf16 v[56:59], a[140:143], v[76:83], v29 abid:1\n\t"
    "ds_read_b64 v[116:117], v216 offset:8192\n\t"
    "v_smfmac_f32_16x16x64_bf16 v[44:47], a[144:147], v[84:91], v30\n\t"
    "ds_read_b64 v[118:119], v217 offset:8192\n\t"
    "v_smfmac_f32_16x16x64_bf16 v[48:51], a[148:151], v[84:91], v30 abid:1\n\t"
    "ds_read_b64 v[120:121], v218 offset:8192\n\t"
    "v_smfmac_f32_16x16x64_bf16 v[52:55], a[152:155], v[84:91], v31\n\t"
    "ds_read_b64 v[122:123], v219 offset:8192\n\t"
    "v_smfmac_f32_16x16x64_bf16 v[56:59], a[156:159], v[84:91], v31 abid:1\n\t"
    "ds_read_b128 v[76:79], v4 offset:9728\n\t"
    "v_smfmac_f32_16x16x64_bf16 v[44:47], a[160:163], v[92:99], v32\n\t"
    "ds_read_b128 v[80:83], v5 offset:9728\n\t"
    "v_smfmac_f32_16x16x64_bf16 v[48:51], a[164:167], v[92:99], v32 abid:1\n\t"
    "ds_read_b128 v[84:87], v2 offset:9984\n\t"
    "v_smfmac_f32_16x16x64_bf16 v[52:55], a[168:171], v[92:99], v33\n\t"
    "ds_read_b128 v[88:91], v3 offset:9984\n\t"
    "v_smfmac_f32_16x16x64_bf16 v[56:59], a[172:175], v[92:99], v33 abid:1\n\t"
    "ds_read_b64 v[124:125], v224 offset:8192\n\t"
    "s_waitcnt lgkmcnt(7)\n\t"
    "v_smfmac_f32_16x16x64_bf16 v[44:47], a[176:179], v[60:67], v34\n\t"
    "ds_read_b128 v[92:95], v4 offset:9984\n\t"
    "v_smfmac_f32_16x16x64_bf16 v[48:51], a[180:183], v[60:67], v34 abid:1\n\t"
    "ds_read_b64 v[126:127], v225 offset:8192\n\t"
    "ds_read_b128 v[96:99], v5 offset:9984\n\t"
    "ds_read_b64 v[128:129], v226 offset:8192\n\t"
    "ds_read_b64 v[130:131], v227 offset:8192\n\t"
    "v_smfmac_f32_16x16x64_bf16 v[52:55], a[184:187], v[60:67], v35\n\t"
    "v_smfmac_f32_16x16x64_bf16 v[56:59], a[188:191], v[60:67], v35 abid:1\n\t"
    "v_smfmac_f32_16x16x64_bf16 v[44:47], a[192:195], v[68:75], v36\n\t"
    "v_smfmac_f32_16x16x64_bf16 v[48:51], a[196:199], v[68:75], v36 abid:1\n\t"
    "v_smfmac_f32_16x16x64_bf16 v[52:55], a[200:203], v[68:75], v37\n\t"
    "v_smfmac_f32_16x16x64_bf16 v[56:59], a[204:207], v[68:75], v37 abid:1\n\t"
    "s_waitcnt vmcnt(10) lgkmcnt(0)\n\t"
    "s_barrier\n\t"
    "ds_read_b128 v[60:63], v2 offset:0\n\t"
    "v_smfmac_f32_16x16x64_bf16 v[44:47], a[208:211], v[76:83], v38\n\t"
    "s_add_u32 m0, s17, 4096\n\t"
    "ds_read_b128 v[64:67], v3 offset:0\n\t"
    "v_smfmac_f32_16x16x64_bf16 v[44:47], a[224:227], v[84:91], v40\n\t"
    "buffer_load_dwordx4 v6, s[8:11], s16 offen offset:0  lds\n\t"
    "ds_read_b128 v[68:71], v4 offset:0\n\t"
    "v_smfmac_f32_16x16x64_bf16 v[44:47], a[240:243], v[92:99], v42\n\t"
    "ds_read_b128 v[72:75], v5 offset:0\n\t"
    "v_smfmac_f32_16x16x64_bf16 v[44:47], v[164:167], v[100:107], v10\n\t"
    "ds_read_b64 v[100:101], v228 offset:0\n\t"
    "v_smfmac_f32_16x16x64_bf16 v[48:51], a[212:215], v[76:83], v38 abid:1\n\t"
    "ds_read_b64 v[102:103], v229 offset:0\n\t"
    "v_smfmac_f32_16x16x64_bf16 v[48:51], a[228:231], v[84:91], v40 abid:1\n\t"
    "ds_read_b64 v[104:105], v230 offset:0\n\t"
    "v_smfmac_f32_16x16x64_bf16 v[48:51], a[244:247], v[92:99], v42 abid:1\n\t"
    "ds_read_b64 v[106:107], v231 offset:0\n\t"
    "v_smfmac_f32_16x16x64_bf16 v[48:51], v[168:171], v[108:115], v10\n\t"
    "ds_read_b64 v[108:109], v204 offset:0\n\t"
    "v_cvt_pk_bf16_f32 v236, v44, v45\n\t"
    "v_smfmac_f32_16x16x64_bf16 v[52:55], a[216:219], v[76:83], v39\n\t"
    "buffer_load_dwordx4 v6, s[8:11], s16 offen offset:1024  lds\n\t"
    "ds_read_b64 v[110:111], v205 offset:0\n\t"
    "v_cvt_pk_bf16_f32 v237, v46, v47\n\t"
    "v_smfmac_f32_16x16x64_bf16 v[52:55], a[232:235], v[84:91], v41\n\t"
    "ds_read_b64 v[112:113], v206 offset:0\n\t"
    "v_mov_b64 v[44:45], v[132:133]\n\t"
    "v_smfmac_f32_16x16x64_bf16 v[52:55], a[248:251], v[92:99], v43\n\t"
    "ds_read_b64 v[114:115], v207 offset:0\n\t"
    "v_mov_b64 v[46:47], v[134:135]\n\t"
    "v_smfmac_f32_16x16x64_bf16 v[52:55], v[172:175], v[116:123], v10\n\t"
    "ds_read_b64 v[116:117], v232 offset:0\n\t"
    "v_cvt_pk_bf16_f32 v238, v48, v49\n\t"
    "v_smfmac_f32_16x16x64_bf16 v[56:59], a[220:223], v[76:83], v39 abid:1\n\t"
    "ds_read_b128 v[76:79], v2 offset:256\n\t"
    "v_cvt_pk_bf16_f32 v239, v50, v51\n\t"
    "v_smfmac_f32_16x16x64_bf16 v[56:59], a[236:239], v[84:91], v41 abid:1\n\t"
    "ds_read_b128 v[80:83], v3 offset:256\n\t"
    "v_mov_b64 v[48:49], v[136:137]\n\t"
    "v_smfmac_f32_16x16x64_bf16 v[56:59], a[252:255], v[92:99], v43 abid:1\n\t"
    "s_add_u32 m0, s17, 14336\n\t"
    "ds_read_b64 v[118:119], v233 offset:0\n\t"
    "v_mov_b64 v[50:51], v[138:139]\n\t"
    "v_smfmac_f32_16x16x64_bf16 v[56:59], v[176:179], v[124:131], v10\n\t"
    "buffer_load_dwordx4 v7, s[8:11], s16 offen offset:0  lds\n\t"
    "s_waitcnt lgkmcnt(6)\n\t"
    "v_cvt_pk_bf16_f32 v240, v52, v53\n\t"
    "v_smfmac_f32_16x16x64_bf16 v[44:47], a[0:3], v[60:67], v12\n\t"
    "ds_read_b64 v[120:121], v234 offset:0\n\t"
    "v_cvt_pk_bf16_f32 v241, v54, v55\n\t"
    "v_smfmac_f32_16x16x64_bf16 v[48:51], a[4:7], v[60:67], v12 abid:1\n\t"
    "ds_read_b64 v[122:123], v235 offset:0\n\t"
    "v_mov_b64 v[52:53], v[140:141]\n\t"
    "v_smfmac_f32_16x16x64_bf16 v[44:47], a[16:19], v[68:75], v14\n\t"
    "ds_read_b128 v[84:87], v4 offset:256\n\t"
    "v_mov_b64 v[54:55], v[142:143]\n\t"
    "v_smfmac_f32_16x16x64_bf16 v[48:51], a[20:23], v[68:75], v14 abid:1\n\t"
    "ds_read_b128 v[88:91], v5 offset:256\n\t"
    "s_waitcnt lgkmcnt(5)\n\t"
    "v_cvt_pk_bf16_f32 v242, v56, v57\n\t"
    "v_smfmac_f32_16x16x64_bf16 v[44:47], a[32:35], v[76:83], v16\n\t"
    "ds_read_b128 v[92:95], v2 offset:512\n\t"
    "v_cvt_pk_bf16_f32 v243, v58, v59\n\t"
    "v_smfmac_f32_16x16x64_bf16 v[52:55], a[8:11], v[60:67], v13\n\t"
    "ds_read_b128 v[96:99], v3 offset:512\n\t"
    "v_mov_b64 v[56:57], v[144:145]\n\t"
    "v_smfmac_f32_16x16x64_bf16 v[48:51], a[36:39], v[76:83], v16 abid:1\n\t"
    "buffer_load_dwordx4 v7, s[8:11], s16 offen offset:1024  lds\n\t"
    "ds_read_b64 v[124:125], v220 offset:0\n\t"
    "v_mov_b64 v[58:59], v[146:147]\n\t"
    "v_smfmac_f32_16x16x64_bf16 v[52:55], a[24:27], v[68:75], v15\n\t"
    "ds_read_b64 v[126:127], v221 offset:0\n\t"
    "v_mov_b32_dpp v244, v240 row_ror:8 row_mask:0xf bank_mask:0xf\n\t"
    "v_smfmac_f32_16x16x64_bf16 v[52:55], a[40:43], v[76:83], v17\n\t"
    "ds_read_b64 v[128:129], v222 offset:0\n\t"
    "v_mov_b32_dpp v245, v241 row_ror:8 row_mask:0xf bank_mask:0xf\n\t"
    "v_smfmac_f32_16x16x64_bf16 v[56:59], a[12:15], v[60:67], v13 abid:1\n\t"
    "ds_read_b128 v[60:63], v4 offset:512\n\t"
    "v_mov_b32_dpp v246, v242 row_ror:8 row_mask:0xf bank_mask:0xf\n\t"
    "v_smfmac_f32_16x16x64_bf16 v[56:59], a[28:31], v[68:75], v15 abid:1\n\t"
    "ds_read_b128 v[64:67], v5 offset:512\n\t"
    "v_mov_b32_dpp v247, v243 row_ror:8 row_mask:0xf bank_mask:0xf\n\t"
    "v_smfmac_f32_16x16x64_bf16 v[56:59], a[44:47], v[76:83], v17 abid:1\n\t"
    "ds_read_b128 v[68:71], v2 offset:768\n\t"
    "v_mov_b32_dpp v240, v236 row_ror:8 row_mask:0xf bank_mask:0x3\n\t"
    "v_smfmac_f32_16x16x64_bf16 v[44:47], v[180:183], v[100:107], v10\n\t"
    "s_add_u32 m0, s17, 24576\n\t"
    "ds_read_b128 v[72:75], v3 offset:768\n\t"
    "v_mov_b32_dpp v241, v237 row_ror:8 row_mask:0xf bank_mask:0x3\n\t"
    "v_smfmac_f32_16x16x64_bf16 v[48:51], v[184:187], v[100:107], v10\n\t"
    "buffer_load_dwordx4 v8, s[8:11], s16 offen offset:0  lds\n\t"
    "s_waitcnt lgkmcnt(6)\n\t"
    "v_mov_b32_dpp v242, v238 row_ror:8 row_mask:0xf bank_mask:0x3\n\t"
    "v_smfmac_f32_16x16x64_bf16 v[52:55], v[188:191], v[116:123], v10\n\t"
    "ds_read_b64 v[100:101], v196 offset:0\n\t"
    "v_mov_b32_dpp v243, v239 row_ror:8 row_mask:0xf bank_mask:0x3\n\t"
    "v_smfmac_f32_16x16x64_bf16 v[56:59], v[192:195], v[116:123], v10\n\t"
    "ds_read_b64 v[102:103], v197 offset:0\n\t"
    "v_mov_b32_dpp v236, v244 quad_perm:[0,1,2,3] row_mask:0xf bank_mask:0xc\n\t"
    "v_smfmac_f32_16x16x64_bf16 v[44:47], a[48:51], v[84:91], v18\n\t"
    "ds_read_b64 v[104:105], v198 offset:0\n\t"
    "v_mov_b32_dpp v237, v245 quad_perm:[0,1,2,3] row_mask:0xf bank_mask:0xc\n\t"
    "v_smfmac_f32_16x16x64_bf16 v[48:51], a[52:55], v[84:91], v18 abid:1\n\t"
    "ds_read_b64 v[106:107], v199 offset:0\n\t"
    "v_mov_b32_dpp v238, v246 quad_perm:[0,1,2,3] row_mask:0xf bank_mask:0xc\n\t"
    "v_smfmac_f32_16x16x64_bf16 v[52:55], a[56:59], v[84:91], v19\n\t"
    "ds_read_b64 v[116:117], v212 offset:0\n\t"
    "v_mov_b32_dpp v239, v247 quad_perm:[0,1,2,3] row_mask:0xf bank_mask:0xc\n\t"
    "v_smfmac_f32_16x16x64_bf16 v[56:59], a[60:63], v[84:91], v19 abid:1\n\t"
    "ds_read_b64 v[118:119], v213 offset:0\n\t"
    "v_smfmac_f32_16x16x64_bf16 v[44:47], a[64:67], v[92:99], v20\n\t"
    "buffer_load_dwordx4 v8, s[8:11], s16 offen offset:1024  lds\n\t"
    "ds_read_b64 v[120:121], v214 offset:0\n\t"
    "v_smfmac_f32_16x16x64_bf16 v[48:51], a[68:71], v[92:99], v20 abid:1\n\t"
    "ds_read_b64 v[122:123], v215 offset:0\n\t"
    "buffer_store_dwordx4 v[236:239], v0, s[48:51], s55 offen sc1\n\t"
    "v_smfmac_f32_16x16x64_bf16 v[52:55], a[72:75], v[92:99], v21\n\t"
    "ds_read_b64 v[130:131], v223 offset:0\n\t"
    "v_smfmac_f32_16x16x64_bf16 v[56:59], a[76:79], v[92:99], v21 abid:1\n\t"
    "ds_read_b128 v[76:79], v4 offset:768\n\t"
    "s_waitcnt lgkmcnt(7)\n\t"
    "v_smfmac_f32_16x16x64_bf16 v[44:47], a[80:83], v[60:67], v22\n\t"
    "ds_read_b128 v[80:83], v5 offset:768\n\t"
    "v_smfmac_f32_16x16x64_bf16 v[48:51], a[84:87], v[60:67], v22 abid:1\n\t"
    "ds_read_b128 v[84:87], v2 offset:1024\n\t"
    "v_smfmac_f32_16x16x64_bf16 v[52:55], a[88:91], v[60:67], v23\n\t"
    "s_add_u32 m0, s17, 34816\n\t"
    "ds_read_b128 v[88:91], v3 offset:1024\n\t"
    "v_smfmac_f32_16x16x64_bf16 v[56:59], a[92:95], v[60:67], v23 abid:1\n\t"
    "buffer_load_dwordx4 v9, s[8:11], s16 offen offset:0  lds\n\t"
    "buffer_store_dwordx4 v[240:243], v1, s[48:51], s55 offen sc1\n\t"
    "ds_read_b128 v[92:95], v4 offset:1024\n\t"
    "v_smfmac_f32_16x16x64_bf16 v[44:47], a[96:99], v[68:75], v24\n\t"
    "ds_read_b128 v[96:99], v5 offset:1024\n\t"
    "s_add_u32 s54, s54, 32768\n\t"
    "s_xor_b32 s55, s54, s53\n\t"
    "v_smfmac_f32_16x16x64_bf16 v[48:51], a[100:103], v[68:75], v24 abid:1\n\t"
    "ds_read_b128 v[60:63], v2 offset:1280\n\t"
    "v_smfmac_f32_16x16x64_bf16 v[52:55], a[104:107], v[68:75], v25\n\t"
    "ds_read_b128 v[64:67], v3 offset:1280\n\t"
    "v_smfmac_f32_16x16x64_bf16 v[56:59], a[108:111], v[68:75], v25 abid:1\n\t"
    "ds_read_b128 v[68:71], v4 offset:1280\n\t"
    "s_waitcnt lgkmcnt(7)\n\t"
    "v_smfmac_f32_16x16x64_bf16 v[44:47], v[148:151], v[100:107], v10\n\t"
    "ds_read_b64 v[100:101], v200 offset:0\n\t"
    "v_smfmac_f32_16x16x64_bf16 v[48:51], v[152:155], v[108:115], v10\n\t"
    "ds_read_b64 v[102:103], v201 offset:0\n\t"
    "v_smfmac_f32_16x16x64_bf16 v[52:55], v[156:159], v[116:123], v10\n\t"
    "buffer_load_dwordx4 v9, s[8:11], s16 offen offset:1024  lds\n\t"
    "s_add_u32 s52, s52, 32768\n\t"
    "s_xor_b32 s16, s52, s53\n\t"
    "ds_read_b64 v[104:105], v202 offset:0\n\t"
    "v_smfmac_f32_16x16x64_bf16 v[56:59], v[160:163], v[124:131], v10\n\t"
    "ds_read_b64 v[106:107], v203 offset:0\n\t"
    "v_smfmac_f32_16x16x64_bf16 v[44:47], a[112:115], v[76:83], v26\n\t"
    "ds_read_b128 v[72:75], v5 offset:1280\n\t"
    "v_smfmac_f32_16x16x64_bf16 v[48:51], a[116:119], v[76:83], v26 abid:1\n\t"
    "ds_read_b64 v[108:109], v208 offset:0\n\t"
    "v_smfmac_f32_16x16x64_bf16 v[52:55], a[120:123], v[76:83], v27\n\t"
    "ds_read_b64 v[110:111], v209 offset:0\n\t"
    "v_smfmac_f32_16x16x64_bf16 v[56:59], a[124:127], v[76:83], v27 abid:1\n\t"
    "ds_read_b64 v[112:113], v210 offset:0\n\t"
    "s_waitcnt lgkmcnt(7)\n\t"
    "v_smfmac_f32_16x16x64_bf16 v[44:47], a[128:131], v[84:91], v28\n\t"
    "ds_read_b64 v[114:115], v211 offset:0\n\t"
    "v_smfmac_f32_16x16x64_bf16 v[48:51], a[132:135], v[84:91], v28 abid:1\n\t"
    "ds_read_b128 v[76:79], v2 offset:1536\n\t"
    "v_smfmac_f32_16x16x64_bf16 v[52:55], a[136:139], v[84:91], v29\n\t"
    "ds_read_b128 v[80:83], v3 offset:1536\n\t"
    "v_smfmac_f32_16x16x64_bf16 v[56:59], a[140:143], v[84:91], v29 abid:1\n\t"
    "ds_read_b64 v[116:117], v216 offset:0\n\t"
    "v_smfmac_f32_16x16x64_bf16 v[44:47], a[144:147], v[92:99], v30\n\t"
    "ds_read_b64 v[118:119], v217 offset:0\n\t"
    "v_smfmac_f32_16x16x64_bf16 v[48:51], a[148:151], v[92:99], v30 abid:1\n\t"
    "ds_read_b64 v[120:121], v218 offset:0\n\t"
    "v_smfmac_f32_16x16x64_bf16 v[52:55], a[152:155], v[92:99], v31\n\t"
    "ds_read_b64 v[122:123], v219 offset:0\n\t"
    "v_smfmac_f32_16x16x64_bf16 v[56:59], a[156:159], v[92:99], v31 abid:1\n\t"
    "ds_read_b128 v[84:87], v4 offset:1536\n\t"
    "v_smfmac_f32_16x16x64_bf16 v[44:47], a[160:163], v[60:67], v32\n\t"
    "ds_read_b128 v[88:91], v5 offset:1536\n\t"
    "v_smfmac_f32_16x16x64_bf16 v[48:51], a[164:167], v[60:67], v32 abid:1\n\t"
    "ds_read_b128 v[92:95], v2 offset:1792\n\t"
    "v_smfmac_f32_16x16x64_bf16 v[52:55], a[168:171], v[60:67], v33\n\t"
    "ds_read_b128 v[96:99], v3 offset:1792\n\t"
    "v_smfmac_f32_16x16x64_bf16 v[56:59], a[172:175], v[60:67], v33 abid:1\n\t"
    "ds_read_b64 v[124:125], v224 offset:0\n\t"
    "s_waitcnt lgkmcnt(7)\n\t"
    "v_smfmac_f32_16x16x64_bf16 v[44:47], a[176:179], v[68:75], v34\n\t"
    "ds_read_b128 v[60:63], v4 offset:1792\n\t"
    "v_smfmac_f32_16x16x64_bf16 v[48:51], a[180:183], v[68:75], v34 abid:1\n\t"
    "ds_read_b64 v[126:127], v225 offset:0\n\t"
    "ds_read_b128 v[64:67], v5 offset:1792\n\t"
    "ds_read_b64 v[128:129], v226 offset:0\n\t"
    "ds_read_b64 v[130:131], v227 offset:0\n\t"
    "v_smfmac_f32_16x16x64_bf16 v[52:55], a[184:187], v[68:75], v35\n\t"
    "v_smfmac_f32_16x16x64_bf16 v[56:59], a[188:191], v[68:75], v35 abid:1\n\t"
    "v_smfmac_f32_16x16x64_bf16 v[44:47], a[192:195], v[76:83], v36\n\t"
    "v_smfmac_f32_16x16x64_bf16 v[48:51], a[196:199], v[76:83], v36 abid:1\n\t"
    "v_smfmac_f32_16x16x64_bf16 v[52:55], a[200:203], v[76:83], v37\n\t"
    "v_smfmac_f32_16x16x64_bf16 v[56:59], a[204:207], v[76:83], v37 abid:1\n\t"
    "s_sub_u32 s18, s18, 1\n\t"
    "s_cmp_lg_u32 s18, 0\n\t"
    "s_waitcnt vmcnt(10) lgkmcnt(0)\n\t"
    "s_barrier\n\t"
    "s_cbranch_scc1 .LloopB_%=\n\t"
    "s_or_b32 s26, %3, %4\n\t"
    "s_cmp_lg_u32 s26, 0\n\t"
    "s_cbranch_scc1 .LnfB_%=\n\t"
    "s_mov_b64 s[44:45], %2\n\t"
    "s_add_u32 s44, s44, 0x1000000\n\t"
    "s_addc_u32 s45, s45, 0\n\t"
    "global_load_dwordx4 v[68:71], v11, s[44:45] offset:0 sc1\n\t"
    "global_load_dwordx4 v[72:75], v11, s[44:45] offset:1024 sc1\n\t"
    "global_load_dwordx4 v[76:79], v11, s[44:45] offset:2048 sc1\n\t"
    "global_load_dwordx4 v[80:83], v11, s[44:45] offset:3072 sc1\n\t"
    ".LnfB_%=:\n\t"
    "v_smfmac_f32_16x16x64_bf16 v[44:47], a[208:211], v[84:91], v38\n\t"
    "v_smfmac_f32_16x16x64_bf16 v[44:47], a[224:227], v[92:99], v40\n\t"
    "v_smfmac_f32_16x16x64_bf16 v[44:47], a[240:243], v[60:67], v42\n\t"
    "v_smfmac_f32_16x16x64_bf16 v[44:47], v[164:167], v[100:107], v10\n\t"
    "v_smfmac_f32_16x16x64_bf16 v[48:51], a[212:215], v[84:91], v38 abid:1\n\t"
    "v_smfmac_f32_16x16x64_bf16 v[48:51], a[228:231], v[92:99], v40 abid:1\n\t"
    "v_smfmac_f32_16x16x64_bf16 v[48:51], a[244:247], v[60:67], v42 abid:1\n\t"
    "v_smfmac_f32_16x16x64_bf16 v[48:51], v[168:171], v[108:115], v10\n\t"
    "s_nop 3\n\t"
    "v_cvt_pk_bf16_f32 v236, v44, v45\n\t"
    "v_smfmac_f32_16x16x64_bf16 v[52:55], a[216:219], v[84:91], v39\n\t"
    "s_nop 0\n\t"
    "v_cvt_pk_bf16_f32 v237, v46, v47\n\t"
    "v_smfmac_f32_16x16x64_bf16 v[52:55], a[232:235], v[92:99], v41\n\t"
    "v_smfmac_f32_16x16x64_bf16 v[52:55], a[248:251], v[60:67], v43\n\t"
    "v_smfmac_f32_16x16x64_bf16 v[52:55], v[172:175], v[116:123], v10\n\t"
    "v_cvt_pk_bf16_f32 v238, v48, v49\n\t"
    "v_smfmac_f32_16x16x64_bf16 v[56:59], a[220:223], v[84:91], v39 abid:1\n\t"
    "v_cvt_pk_bf16_f32 v239, v50, v51\n\t"
    "v_smfmac_f32_16x16x64_bf16 v[56:59], a[236:239], v[92:99], v41 abid:1\n\t"
    "v_smfmac_f32_16x16x64_bf16 v[56:59], a[252:255], v[60:67], v43 abid:1\n\t"
    "v_smfmac_f32_16x16x64_bf16 v[56:59], v[176:179], v[124:131], v10\n\t"
    "s_nop 1\n\t"
    "v_cvt_pk_bf16_f32 v240, v52, v53\n\t"
    "v_cvt_pk_bf16_f32 v241, v54, v55\n\t"
    "s_nop 4\n\t"
    "v_cvt_pk_bf16_f32 v242, v56, v57\n\t"
    "s_nop 2\n\t"
    "v_cvt_pk_bf16_f32 v243, v58, v59\n\t"
    "s_nop 1\n\t"
    "v_mov_b32_dpp v244, v240 row_ror:8 row_mask:0xf bank_mask:0xf\n\t"
    "v_mov_b32_dpp v245, v241 row_ror:8 row_mask:0xf bank_mask:0xf\n\t"
    "v_mov_b32_dpp v246, v242 row_ror:8 row_mask:0xf bank_mask:0xf\n\t"
    "v_mov_b32_dpp v247, v243 row_ror:8 row_mask:0xf bank_mask:0xf\n\t"
    "v_mov_b32_dpp v240, v236 row_ror:8 row_mask:0xf bank_mask:0x3\n\t"
    "v_mov_b32_dpp v241, v237 row_ror:8 row_mask:0xf bank_mask:0x3\n\t"
    "v_mov_b32_dpp v242, v238 row_ror:8 row_mask:0xf bank_mask:0x3\n\t"
    "v_mov_b32_dpp v243, v239 row_ror:8 row_mask:0xf bank_mask:0x3\n\t"
    "v_mov_b32_dpp v236, v244 quad_perm:[0,1,2,3] row_mask:0xf bank_mask:0xc\n\t"
    "v_mov_b32_dpp v237, v245 quad_perm:[0,1,2,3] row_mask:0xf bank_mask:0xc\n\t"
    "v_mov_b32_dpp v238, v246 quad_perm:[0,1,2,3] row_mask:0xf bank_mask:0xc\n\t"
    "v_mov_b32_dpp v239, v247 quad_perm:[0,1,2,3] row_mask:0xf bank_mask:0xc\n\t"
    "buffer_store_dwordx4 v[236:239], v0, s[48:51], s55 offen sc1\n\t"
    "buffer_store_dwordx4 v[240:243], v1, s[48:51], s55 offen sc1\n\t"
    "s_add_u32 s54, s54, 32768\n\t"
    "s_xor_b32 s55, s54, s53\n\t"
    "s_mov_b32 s47, 1\n\t"
    "s_branch .Lend_%=\n\t"
    ".LvarC_%=:\n\t"
    "s_add_u32 m0, s17, 6144\n\t"
    "s_add_u32 s40, s53, 98304\n\t"
    "buffer_load_dwordx4 v6, s[8:11], s40 offen offset:1024  lds\n\t"
    "s_add_u32 m0, s17, 16384\n\t"
    "s_add_u32 s40, s53, 98304\n\t"
    "buffer_load_dwordx4 v7, s[8:11], s40 offen offset:1024  lds\n\t"
    "s_add_u32 m0, s17, 26624\n\t"
    "s_add_u32 s40, s53, 98304\n\t"
    "buffer_load_dwordx4 v8, s[8:11], s40 offen offset:1024  lds\n\t"
    "s_add_u32 m0, s17, 36864\n\t"
    "s_add_u32 s40, s53, 98304\n\t"
    "buffer_load_dwordx4 v9, s[8:11], s40 offen offset:1024  lds\n\t"
    "global_load_dwordx4 a[64:67], v11, s[30:31] offset:0 sc1\n\t"
    "global_load_dwordx4 a[68:71], v11, s[30:31] offset:1024 sc1\n\t"
    "global_load_dwordx4 a[72:75], v11, s[30:31] offset:2048 sc1\n\t"
    "global_load_dwordx4 a[76:79], v11, s[30:31] offset:3072 sc1\n\t"
    "s_add_u32 s30, s30, 4096\n\t"
    "s_addc_u32 s31, s31, 0\n\t"
    "global_load_dwordx4 a[80:83], v11, s[30:31] offset:0 sc1\n\t"
    "global_load_dwordx4 a[84:87], v11, s[30:31] offset:1024 sc1\n\t"
    "global_load_dwordx4 a[88:91], v11, s[30:31] offset:2048 sc1\n\t"
    "global_load_dwordx4 a[92:95], v11, s[30:31] offset:3072 sc1\n\t"
    "s_add_u32 s30, s30, 4096\n\t"
    "s_addc_u32 s31, s31, 0\n\t"
    "global_load_dwordx4 a[96:99], v11, s[30:31] offset:0 sc1\n\t"
    "global_load_dwordx4 a[100:103], v11, s[30:31] offset:1024 sc1\n\t"
    "global_load_dwordx4 a[104:107], v11, s[30:31] offset:2048 sc1\n\t"
    "global_load_dwordx4 a[108:111], v11, s[30:31] offset:3072 sc1\n\t"
    "s_add_u32 s30, s30, 4096\n\t"
    "s_addc_u32 s31, s31, 0\n\t"
    "global_load_dwordx4 a[112:115], v11, s[30:31] offset:0 sc1\n\t"
    "global_load_dwordx4 a[116:119], v11, s[30:31] offset:1024 sc1\n\t"
    "global_load_dwordx4 a[120:123], v11, s[30:31] offset:2048 sc1\n\t"
    "global_load_dwordx4 a[124:127], v11, s[30:31] offset:3072 sc1\n\t"
    "s_add_u32 s30, s30, 4096\n\t"
    "s_addc_u32 s31, s31, 0\n\t"
    "global_load_dwordx4 v[28:31], v11, s[36:37] offset:0 sc1\n\t"
    "global_load_dwordx4 v[32:35], v11, s[36:37] offset:1024 sc1\n\t"
    "global_load_dwordx4 v[36:39], v11, s[36:37] offset:2048 sc1\n\t"
    "global_load_dwordx4 v[40:43], v11, s[36:37] offset:3072 sc1\n\t"
    "global_load_dwordx4 a[128:131], v11, s[30:31] offset:0 sc1\n\t"
    "global_load_dwordx4 a[132:135], v11, s[30:31] offset:1024 sc1\n\t"
    "global_load_dwordx4 a[136:139], v11, s[30:31] offset:2048 sc1\n\t"
    "global_load_dwordx4 a[140:143], v11, s[30:31] offset:3072 sc1\n\t"
    "s_add_u32 s30, s30, 4096\n\t"
    "s_addc_u32 s31, s31, 0\n\t"
    "global_load_dwordx4 a[144:147], v11, s[30:31] offset:0 sc1\n\t"
    "global_load_dwordx4 a[148:151], v11, s[30:31] offset:1024 sc1\n\t"
    "global_load_dwordx4 a[152:155], v11, s[30:31] offset:2048 sc1\n\t"
    "global_load_dwordx4 a[156:159], v11, s[30:31] offset:3072 sc1\n\t"
    "s_add_u32 s30, s30, 4096\n\t"
    "s_addc_u32 s31, s31, 0\n\t"
    "global_load_dwordx4 a[160:163], v11, s[30:31] offset:0 sc1\n\t"
    "global_load_dwordx4 a[164:167], v11, s[30:31] offset:1024 sc1\n\t"
    "global_load_dwordx4 a[168:171], v11, s[30:31] offset:2048 sc1\n\t"
    "global_load_dwordx4 a[172:175], v11, s[30:31] offset:3072 sc1\n\t"
    "s_add_u32 s30, s30, 4096\n\t"
    "s_addc_u32 s31, s31, 0\n\t"
    "global_load_dwordx4 a[176:179], v11, s[30:31] offset:0 sc1\n\t"
    "global_load_dwordx4 a[180:183], v11, s[30:31] offset:1024 sc1\n\t"
    "global_load_dwordx4 a[184:187], v11, s[30:31] offset:2048 sc1\n\t"
    "global_load_dwordx4 a[188:191], v11, s[30:31] offset:3072 sc1\n\t"
    "s_add_u32 s30, s30, 4096\n\t"
    "s_addc_u32 s31, s31, 0\n\t"
    "global_load_dwordx4 a[192:195], v11, s[30:31] offset:0 sc1\n\t"
    "global_load_dwordx4 a[196:199], v11, s[30:31] offset:1024 sc1\n\t"
    "global_load_dwordx4 a[200:203], v11, s[30:31] offset:2048 sc1\n\t"
    "global_load_dwordx4 a[204:207], v11, s[30:31] offset:3072 sc1\n\t"
    "s_add_u32 s30, s30, 4096\n\t"
    "s_addc_u32 s31, s31, 0\n\t"
    "global_load_dwordx4 a[208:211], v11, s[30:31] offset:0 sc1\n\t"
    "global_load_dwordx4 a[212:215], v11, s[30:31] offset:1024 sc1\n\t"
    "global_load_dwordx4 a[216:219], v11, s[30:31] offset:2048 sc1\n\t"
    "global_load_dwordx4 a[220:223], v11, s[30:31] offset:3072 sc1\n\t"
    "s_add_u32 s30, s30, 4096\n\t"
    "s_addc_u32 s31, s31, 0\n\t"
    "global_load_dwordx4 a[224:227], v11, s[30:31] offset:0 sc1\n\t"
    "global_load_dwordx4 a[228:231], v11, s[30:31] offset:1024 sc1\n\t"
    "global_load_dwordx4 a[232:235], v11, s[30:31] offset:2048 sc1\n\t"
    "global_load_dwordx4 a[236:239], v11, s[30:31] offset:3072 sc1\n\t"
    "s_add_u32 s30, s30, 4096\n\t"
    "s_addc_u32 s31, s31, 0\n\t"
    "global_load_dwordx4 a[240:243], v11, s[30:31] offset:0 sc1\n\t"
    "global_load_dwordx4 a[244:247], v11, s[30:31] offset:1024 sc1\n\t"
    "global_load_dwordx4 a[248:251], v11, s[30:31] offset:2048 sc1\n\t"
    "global_load_dwordx4 a[252:255], v11, s[30:31] offset:3072 sc1\n\t"
    "s_add_u32 s30, s30, 4096\n\t"
    "s_addc_u32 s31, s31, 0\n\t"
    "s_add_u32 m0, s17, 8192\n\t"
    "s_nop 0\n\t"
    "buffer_load_dwordx4 v6, s[8:11], s16 offen offset:0  lds\n\t"
    "buffer_load_dwordx4 v6, s[8:11], s16 offen offset:1024  lds\n\t"
    "s_add_u32 m0, s17, 18432\n\t"
    "s_nop 0\n\t"
    "buffer_load_dwordx4 v7, s[8:11], s16 offen offset:0  lds\n\t"
    "buffer_load_dwordx4 v7, s[8:11], s16 offen offset:1024  lds\n\t"
    "s_add_u32 m0, s17, 28672\n\t"
    "s_nop 0\n\t"
    "buffer_load_dwordx4 v8, s[8:11], s16 offen offset:0  lds\n\t"
    "buffer_load_dwordx4 v8, s[8:11], s16 offen offset:1024  lds\n\t"
    "s_add_u32 m0, s17, 38912\n\t"
    "s_nop 0\n\t"
    "buffer_load_dwordx4 v9, s[8:11], s16 offen offset:0  lds\n\t"
    "buffer_load_dwordx4 v9, s[8:11], s16 offen offset:1024  lds\n\t"
    "s_add_u32 s52, s52, 32768\n\t"
    "s_xor_b32 s16, s52, s53\n\t"
    "s_mov_b32 s22, 0\n\t"
    "s_mov_b32 s23, 0\n\t"
    "s_mov_b32 s18, 0\n\t"
    "s_waitcnt vmcnt(0)\n\t"
    "s_barrier\n\t"
    ".LloopC_%=:\n\t"
    "s_cmp_lt_u32 s18, 3\n\t"
    "s_cbranch_scc1 .LCnodma_%=\n\t"
    "s_cmp_eq_u32 s18, 3\n\t"
    "s_cselect_b32 s27, 1, 1\n\t"
    ".LCdma_%=:\n\t"
    "s_add_u32 s26, s17, s23\n\t"
    "s_add_u32 m0, s26, 0\n\t"
    "s_nop 0\n\t"
    "buffer_load_dwordx4 v6, s[8:11], s16 offen offset:0  lds\n\t"
    "buffer_load_dwordx4 v6, s[8:11], s16 offen offset:1024  lds\n\t"
    "s_add_u32 s26, s17, s23\n\t"
    "s_add_u32 m0, s26, 10240\n\t"
    "s_nop 0\n\t"
    "buffer_load_dwordx4 v7, s[8:11], s16 offen offset:0  lds\n\t"
    "buffer_load_dwordx4 v7, s[8:11], s16 offen offset:1024  lds\n\t"
    "s_add_u32 s26, s17, s23\n\t"
    "s_add_u32 m0, s26, 20480\n\t"
    "s_nop 0\n\t"
    "buffer_load_dwordx4 v8, s[8:11], s16 offen offset:0  lds\n\t"
    "buffer_load_dwordx4 v8, s[8:11], s16 offen offset:1024  lds\n\t"
    "s_add_u32 s26, s17, s23\n\t"
    "s_add_u32 m0, s26, 30720\n\t"
    "s_nop 0\n\t"
    "buffer_load_dwordx4 v9, s[8:11], s16 offen offset:0  lds\n\t"
    "buffer_load_dwordx4 v9, s[8:11], s16 offen offset:1024  lds\n\t"
    "s_add_u32 s52, s52, 32768\n\t"
    "s_xor_b32 s16, s52, s53\n\t"
    "s_add_u32 s23, s23, 2048\n\t"
    "s_cmp_eq_u32 s23, 10240\n\t"
    "s_cselect_b32 s23, 0, s23\n\t"
    "s_sub_u32 s27, s27, 1\n\t"
    "s_cmp_lg_u32 s27, 0\n\t"
    "s_cbranch_scc1 .LCdma_%=\n\t"
    ".LCnodma_%=:\n\t"
    "v_add_u32 v92, s22, v2\n\t"
    "v_add_u32 v93, s22, v3\n\t"
    "v_add_u32 v94, s22, v4\n\t"
    "v_add_u32 v95, s22, v5\n\t"
    "v_mov_b64 v[44:45], v[132:133]\n\t"
    "v_mov_b64 v[46:47], v[134:135]\n\t"
    "v_mov_b64 v[48:49], v[136:137]\n\t"
    "v_mov_b64 v[50:51], v[138:139]\n\t"
    "v_mov_b64 v[52:53], v[140:141]\n\t"
    "v_mov_b64 v[54:55], v[142:143]\n\t"
    "v_mov_b64 v[56:57], v[144:145]\n\t"
    "v_mov_b64 v[58:59], v[146:147]\n\t"
    "s_mov_b64 s[24:25], s[20:21]\n\t"
    "global_load_dwordx4 v[148:151], v11, s[24:25] offset:0 sc1\n\t"
    "global_load_dwordx4 v[152:155], v11, s[24:25] offset:1024 sc1\n\t"
    "global_load_dwordx4 v[156:159], v11, s[24:25] offset:2048 sc1\n\t"
    "global_load_dwordx4 v[160:163], v11, s[24:25] offset:3072 sc1\n\t"
    "s_add_u32 s24, s24, 4096\n\t"
    "s_addc_u32 s25, s25, 0\n\t"
    "global_load_dwordx4 v[164:167], v11, s[24:25] offset:0 sc1\n\t"
    "global_load_dwordx4 v[168:171], v11, s[24:25] offset:1024 sc1\n\t"
    "global_load_dwordx4 v[172:175], v11, s[24:25] offset:2048 sc1\n\t"
    "global_load_dwordx4 v[176:179], v11, s[24:25] offset:3072 sc1\n\t"
    "s_add_u32 s24, s24, 4096\n\t"
    "s_addc_u32 s25, s25, 0\n\t"
    "ds_read_b128 v[60:63], v92 offset:0\n\t"
    "ds_read_b128 v[64:67], v93 offset:0\n\t"
    "ds_read_b128 v[68:71], v94 offset:0\n\t"
    "ds_read_b128 v[72:75], v95 offset:0\n\t"
    "s_waitcnt lgkmcnt(0)\n\t"
    "v_smfmac_f32_16x16x64_bf16 v[44:47], a[0:3], v[60:67], v12\n\t"
    "v_smfmac_f32_16x16x64_bf16 v[48:51], a[4:7], v[60:67], v12 abid:1\n\t"
    "v_smfmac_f32_16x16x64_bf16 v[52:55], a[8:11], v[60:67], v13\n\t"
    "v_smfmac_f32_16x16x64_bf16 v[56:59], a[12:15], v[60:67], v13 abid:1\n\t"
    "v_smfmac_f32_16x16x64_bf16 v[44:47], a[16:19], v[68:75], v14\n\t"
    "v_smfmac_f32_16x16x64_bf16 v[48:51], a[20:23], v[68:75], v14 abid:1\n\t"
    "v_smfmac_f32_16x16x64_bf16 v[52:55], a[24:27], v[68:75], v15\n\t"
    "v_smfmac_f32_16x16x64_bf16 v[56:59], a[28:31], v[68:75], v15 abid:1\n\t"
    "s_waitcnt vmcnt(0)\n\t"
    "v_smfmac_f32_16x16x64_bf16 v[44:47], v[148:151], v[60:67], v10\n\t"
    "v_smfmac_f32_16x16x64_bf16 v[48:51], v[152:155], v[60:67], v10\n\t"
    "v_smfmac_f32_16x16x64_bf16 v[52:55], v[156:159], v[60:67], v10\n\t"
    "v_smfmac_f32_16x16x64_bf16 v[56:59], v[160:163], v[60:67], v10\n\t"
    "v_smfmac_f32_16x16x64_bf16 v[44:47], v[164:167], v[68:75], v10\n\t"
    "v_smfmac_f32_16x16x64_bf16 v[48:51], v[168:171], v[68:75], v10\n\t"
    "v_smfmac_f32_16x16x64_bf16 v[52:55], v[172:175], v[68:75], v10\n\t"
    "v_smfmac_f32_16x16x64_bf16 v[56:59], v[176:179], v[68:75], v10\n\t"
    "s_nop 3\n\t"
    "global_load_dwordx4 v[148:151], v11, s[24:25] offset:0 sc1\n\t"
    "global_load_dwordx4 v[152:155], v11, s[24:25] offset:1024 sc1\n\t"
    "global_load_dwordx4 v[156:159], v11, s[24:25] offset:2048 sc1\n\t"
    "global_load_dwordx4 v[160:163], v11, s[24:25] offset:3072 sc1\n\t"
    "s_add_u32 s24, s24, 4096\n\t"
    "s_addc_u32 s25, s25, 0\n\t"
    "global_load_dwordx4 v[164:167], v11, s[24:25] offset:0 sc1\n\t"
    "global_load_dwordx4 v[168:171], v11, s[24:25] offset:1024 sc1\n\t"
    "global_load_dwordx4 v[172:175], v11, s[24:25] offset:2048 sc1\n\t"
    "global_load_dwordx4 v[176:179], v11, s[24:25] offset:3072 sc1\n\t"
    "s_add_u32 s24, s24, 4096\n\t"
    "s_addc_u32 s25, s25, 0\n\t"
    "ds_read_b128 v[60:63], v92 offset:256\n\t"
    "ds_read_b128 v[64:67], v93 offset:256\n\t"
    "ds_read_b128 v[68:71], v94 offset:256\n\t"
    "ds_read_b128 v[72:75], v95 offset:256\n\t"
    "s_waitcnt lgkmcnt(0)\n\t"
    "v_smfmac_f32_16x16x64_bf16 v[44:47], a[32:35], v[60:67], v16\n\t"
    "v_smfmac_f32_16x16x64_bf16 v[48:51], a[36:39], v[60:67], v16 abid:1\n\t"
    "v_smfmac_f32_16x16x64_bf16 v[52:55], a[40:43], v[60:67], v17\n\t"
    "v_smfmac_f32_16x16x64_bf16 v[56:59], a[44:47], v[60:67], v17 abid:1\n\t"
    "v_smfmac_f32_16x16x64_bf16 v[44:47], a[48:51], v[68:75], v18\n\t"
    "v_smfmac_f32_16x16x64_bf16 v[48:51], a[52:55], v[68:75], v18 abid:1\n\t"
    "v_smfmac_f32_16x16x64_bf16 v[52:55], a[56:59], v[68:75], v19\n\t"
    "v_smfmac_f32_16x16x64_bf16 v[56:59], a[60:63], v[68:75], v19 abid:1\n\t"
    "s_waitcnt vmcnt(0)\n\t"
    "v_smfmac_f32_16x16x64_bf16 v[44:47], v[148:151], v[60:67], v10\n\t"
    "v_smfmac_f32_16x16x64_bf16 v[48:51], v[152:155], v[60:67], v10\n\t"
    "v_smfmac_f32_16x16x64_bf16 v[52:55], v[156:159], v[60:67], v10\n\t"
    "v_smfmac_f32_16x16x64_bf16 v[56:59], v[160:163], v[60:67], v10\n\t"
    "v_smfmac_f32_16x16x64_bf16 v[44:47], v[164:167], v[68:75], v10\n\t"
    "v_smfmac_f32_16x16x64_bf16 v[48:51], v[168:171], v[68:75], v10\n\t"
    "v_smfmac_f32_16x16x64_bf16 v[52:55], v[172:175], v[68:75], v10\n\t"
    "v_smfmac_f32_16x16x64_bf16 v[56:59], v[176:179], v[68:75], v10\n\t"
    "s_nop 3\n\t"
    "global_load_dwordx4 v[148:151], v11, s[24:25] offset:0 sc1\n\t"
    "global_load_dwordx4 v[152:155], v11, s[24:25] offset:1024 sc1\n\t"
    "global_load_dwordx4 v[156:159], v11, s[24:25] offset:2048 sc1\n\t"
    "global_load_dwordx4 v[160:163], v11, s[24:25] offset:3072 sc1\n\t"
    "s_add_u32 s24, s24, 4096\n\t"
    "s_addc_u32 s25, s25, 0\n\t"
    "global_load_dwordx4 v[164:167], v11, s[24:25] offset:0 sc1\n\t"
    "global_load_dwordx4 v[168:171], v11, s[24:25] offset:1024 sc1\n\t"
    "global_load_dwordx4 v[172:175], v11, s[24:25] offset:2048 sc1\n\t"
    "global_load_dwordx4 v[176:179], v11, s[24:25] offset:3072 sc1\n\t"
    "s_add_u32 s24, s24, 4096\n\t"
    "s_addc_u32 s25, s25, 0\n\t"
    "ds_read_b128 v[60:63], v92 offset:512\n\t"
    "ds_read_b128 v[64:67], v93 offset:512\n\t"
    "ds_read_b128 v[68:71], v94 offset:512\n\t"
    "ds_read_b128 v[72:75], v95 offset:512\n\t"
    "s_waitcnt lgkmcnt(0)\n\t"
    "v_smfmac_f32_16x16x64_bf16 v[44:47], a[64:67], v[60:67], v20\n\t"
    "v_smfmac_f32_16x16x64_bf16 v[48:51], a[68:71], v[60:67], v20 abid:1\n\t"
    "v_smfmac_f32_16x16x64_bf16 v[52:55], a[72:75], v[60:67], v21\n\t"
    "v_smfmac_f32_16x16x64_bf16 v[56:59], a[76:79], v[60:67], v21 abid:1\n\t"
    "v_smfmac_f32_16x16x64_bf16 v[44:47], a[80:83], v[68:75], v22\n\t"
    "v_smfmac_f32_16x16x64_bf16 v[48:51], a[84:87], v[68:75], v22 abid:1\n\t"
    "v_smfmac_f32_16x16x64_bf16 v[52:55], a[88:91], v[68:75], v23\n\t"
    "v_smfmac_f32_16x16x64_bf16 v[56:59], a[92:95], v[68:75], v23 abid:1\n\t"
    "s_waitcnt vmcnt(0)\n\t"
    "v_smfmac_f32_16x16x64_bf16 v[44:47], v[148:151], v[60:67], v10\n\t"
    "v_smfmac_f32_16x16x64_bf16 v[48:51], v[152:155], v[60:67], v10\n\t"
    "v_smfmac_f32_16x16x64_bf16 v[52:55], v[156:159], v[60:67], v10\n\t"
    "v_smfmac_f32_16x16x64_bf16 v[56:59], v[160:163], v[60:67], v10\n\t"
    "v_smfmac_f32_16x16x64_bf16 v[44:47], v[164:167], v[68:75], v10\n\t"
    "v_smfmac_f32_16x16x64_bf16 v[48:51], v[168:171], v[68:75], v10\n\t"
    "v_smfmac_f32_16x16x64_bf16 v[52:55], v[172:175], v[68:75], v10\n\t"
    "v_smfmac_f32_16x16x64_bf16 v[56:59], v[176:179], v[68:75], v10\n\t"
    "s_nop 3\n\t"
    "global_load_dwordx4 v[148:151], v11, s[24:25] offset:0 sc1\n\t"
    "global_load_dwordx4 v[152:155], v11, s[24:25] offset:1024 sc1\n\t"
    "global_load_dwordx4 v[156:159], v11, s[24:25] offset:2048 sc1\n\t"
    "global_load_dwordx4 v[160:163], v11, s[24:25] offset:3072 sc1\n\t"
    "s_add_u32 s24, s24, 4096\n\t"
    "s_addc_u32 s25, s25, 0\n\t"
    "global_load_dwordx4 v[164:167], v11, s[24:25] offset:0 sc1\n\t"
    "global_load_dwordx4 v[168:171], v11, s[24:25] offset:1024 sc1\n\t"
    "global_load_dwordx4 v[172:175], v11, s[24:25] offset:2048 sc1\n\t"
    "global_load_dwordx4 v[176:179], v11, s[24:25] offset:3072 sc1\n\t"
    "s_add_u32 s24, s24, 4096\n\t"
    "s_addc_u32 s25, s25, 0\n\t"
    "ds_read_b128 v[60:63], v92 offset:768\n\t"
    "ds_read_b128 v[64:67], v93 offset:768\n\t"
    "ds_read_b128 v[68:71], v94 offset:768\n\t"
    "ds_read_b128 v[72:75], v95 offset:768\n\t"
    "s_waitcnt lgkmcnt(0)\n\t"
    "v_smfmac_f32_16x16x64_bf16 v[44:47], a[96:99], v[60:67], v24\n\t"
    "v_smfmac_f32_16x16x64_bf16 v[48:51], a[100:103], v[60:67], v24 abid:1\n\t"
    "v_smfmac_f32_16x16x64_bf16 v[52:55], a[104:107], v[60:67], v25\n\t"
    "v_smfmac_f32_16x16x64_bf16 v[56:59], a[108:111], v[60:67], v25 abid:1\n\t"
    "v_smfmac_f32_16x16x64_bf16 v[44:47], a[112:115], v[68:75], v26\n\t"
    "v_smfmac_f32_16x16x64_bf16 v[48:51], a[116:119], v[68:75], v26 abid:1\n\t"
    "v_smfmac_f32_16x16x64_bf16 v[52:55], a[120:123], v[68:75], v27\n\t"
    "v_smfmac_f32_16x16x64_bf16 v[56:59], a[124:127], v[68:75], v27 abid:1\n\t"
    "s_waitcnt vmcnt(0)\n\t"
    "v_smfmac_f32_16x16x64_bf16 v[44:47], v[148:151], v[60:67], v10\n\t"
    "v_smfmac_f32_16x16x64_bf16 v[48:51], v[152:155], v[60:67], v10\n\t"
    "v_smfmac_f32_16x16x64_bf16 v[52:55], v[156:159], v[60:67], v10\n\t"
    "v_smfmac_f32_16x16x64_bf16 v[56:59], v[160:163], v[60:67], v10\n\t"
    "v_smfmac_f32_16x16x64_bf16 v[44:47], v[164:167], v[68:75], v10\n\t"
    "v_smfmac_f32_16x16x64_bf16 v[48:51], v[168:171], v[68:75], v10\n\t"
    "v_smfmac_f32_16x16x64_bf16 v[52:55], v[172:175], v[68:75], v10\n\t"
    "v_smfmac_f32_16x16x64_bf16 v[56:59], v[176:179], v[68:75], v10\n\t"
    "s_nop 3\n\t"
    "global_load_dwordx4 v[148:151], v11, s[24:25] offset:0 sc1\n\t"
    "global_load_dwordx4 v[152:155], v11, s[24:25] offset:1024 sc1\n\t"
    "global_load_dwordx4 v[156:159], v11, s[24:25] offset:2048 sc1\n\t"
    "global_load_dwordx4 v[160:163], v11, s[24:25] offset:3072 sc1\n\t"
    "s_add_u32 s24, s24, 4096\n\t"
    "s_addc_u32 s25, s25, 0\n\t"
    "global_load_dwordx4 v[164:167], v11, s[24:25] offset:0 sc1\n\t"
    "global_load_dwordx4 v[168:171], v11, s[24:25] offset:1024 sc1\n\t"
    "global_load_dwordx4 v[172:175], v11, s[24:25] offset:2048 sc1\n\t"
    "global_load_dwordx4 v[176:179], v11, s[24:25] offset:3072 sc1\n\t"
    "s_add_u32 s24, s24, 4096\n\t"
    "s_addc_u32 s25, s25, 0\n\t"
    "ds_read_b128 v[60:63], v92 offset:1024\n\t"
    "ds_read_b128 v[64:67], v93 offset:1024\n\t"
    "ds_read_b128 v[68:71], v94 offset:1024\n\t"
    "ds_read_b128 v[72:75], v95 offset:1024\n\t"
    "s_waitcnt lgkmcnt(0)\n\t"
    "v_smfmac_f32_16x16x64_bf16 v[44:47], a[128:131], v[60:67], v28\n\t"
    "v_smfmac_f32_16x16x64_bf16 v[48:51], a[132:135], v[60:67], v28 abid:1\n\t"
    "v_smfmac_f32_16x16x64_bf16 v[52:55], a[136:139], v[60:67], v29\n\t"
    "v_smfmac_f32_16x16x64_bf16 v[56:59], a[140:143], v[60:67], v29 abid:1\n\t"
    "v_smfmac_f32_16x16x64_bf16 v[44:47], a[144:147], v[68:75], v30\n\t"
    "v_smfmac_f32_16x16x64_bf16 v[48:51], a[148:151], v[68:75], v30 abid:1\n\t"
    "v_smfmac_f32_16x16x64_bf16 v[52:55], a[152:155], v[68:75], v31\n\t"
    "v_smfmac_f32_16x16x64_bf16 v[56:59], a[156:159], v[68:75], v31 abid:1\n\t"
    "s_waitcnt vmcnt(0)\n\t"
    "v_smfmac_f32_16x16x64_bf16 v[44:47], v[148:151], v[60:67], v10\n\t"
    "v_smfmac_f32_16x16x64_bf16 v[48:51], v[152:155], v[60:67], v10\n\t"
    "v_smfmac_f32_16x16x64_bf16 v[52:55], v[156:159], v[60:67], v10\n\t"
    "v_smfmac_f32_16x16x64_bf16 v[56:59], v[160:163], v[60:67], v10\n\t"
    "v_smfmac_f32_16x16x64_bf16 v[44:47], v[164:167], v[68:75], v10\n\t"
    "v_smfmac_f32_16x16x64_bf16 v[48:51], v[168:171], v[68:75], v10\n\t"
    "v_smfmac_f32_16x16x64_bf16 v[52:55], v[172:175], v[68:75], v10\n\t"
    "v_smfmac_f32_16x16x64_bf16 v[56:59], v[176:179], v[68:75], v10\n\t"
    "s_nop 3\n\t"
    "global_load_dwordx4 v[148:151], v11, s[24:25] offset:0 sc1\n\t"
    "global_load_dwordx4 v[152:155], v11, s[24:25] offset:1024 sc1\n\t"
    "global_load_dwordx4 v[156:159], v11, s[24:25] offset:2048 sc1\n\t"
    "global_load_dwordx4 v[160:163], v11, s[24:25] offset:3072 sc1\n\t"
    "s_add_u32 s24, s24, 4096\n\t"
    "s_addc_u32 s25, s25, 0\n\t"
    "global_load_dwordx4 v[164:167], v11, s[24:25] offset:0 sc1\n\t"
    "global_load_dwordx4 v[168:171], v11, s[24:25] offset:1024 sc1\n\t"
    "global_load_dwordx4 v[172:175], v11, s[24:25] offset:2048 sc1\n\t"
    "global_load_dwordx4 v[176:179], v11, s[24:25] offset:3072 sc1\n\t"
    "s_add_u32 s24, s24, 4096\n\t"
    "s_addc_u32 s25, s25, 0\n\t"
    "ds_read_b128 v[60:63], v92 offset:1280\n\t"
    "ds_read_b128 v[64:67], v93 offset:1280\n\t"
    "ds_read_b128 v[68:71], v94 offset:1280\n\t"
    "ds_read_b128 v[72:75], v95 offset:1280\n\t"
    "s_waitcnt lgkmcnt(0)\n\t"
    "v_smfmac_f32_16x16x64_bf16 v[44:47], a[160:163], v[60:67], v32\n\t"
    "v_smfmac_f32_16x16x64_bf16 v[48:51], a[164:167], v[60:67], v32 abid:1\n\t"
    "v_smfmac_f32_16x16x64_bf16 v[52:55], a[168:171], v[60:67], v33\n\t"
    "v_smfmac_f32_16x16x64_bf16 v[56:59], a[172:175], v[60:67], v33 abid:1\n\t"
    "v_smfmac_f32_16x16x64_bf16 v[44:47], a[176:179], v[68:75], v34\n\t"
    "v_smfmac_f32_16x16x64_bf16 v[48:51], a[180:183], v[68:75], v34 abid:1\n\t"
    "v_smfmac_f32_16x16x64_bf16 v[52:55], a[184:187], v[68:75], v35\n\t"
    "v_smfmac_f32_16x16x64_bf16 v[56:59], a[188:191], v[68:75], v35 abid:1\n\t"
    "s_waitcnt vmcnt(0)\n\t"
    "v_smfmac_f32_16x16x64_bf16 v[44:47], v[148:151], v[60:67], v10\n\t"
    "v_smfmac_f32_16x16x64_bf16 v[48:51], v[152:155], v[60:67], v10\n\t"
    "v_smfmac_f32_16x16x64_bf16 v[52:55], v[156:159], v[60:67], v10\n\t"
    "v_smfmac_f32_16x16x64_bf16 v[56:59], v[160:163], v[60:67], v10\n\t"
    "v_smfmac_f32_16x16x64_bf16 v[44:47], v[164:167], v[68:75], v10\n\t"
    "v_smfmac_f32_16x16x64_bf16 v[48:51], v[168:171], v[68:75], v10\n\t"
    "v_smfmac_f32_16x16x64_bf16 v[52:55], v[172:175], v[68:75], v10\n\t"
    "v_smfmac_f32_16x16x64_bf16 v[56:59], v[176:179], v[68:75], v10\n\t"
    "s_nop 3\n\t"
    "global_load_dwordx4 v[148:151], v11, s[24:25] offset:0 sc1\n\t"
    "global_load_dwordx4 v[152:155], v11, s[24:25] offset:1024 sc1\n\t"
    "global_load_dwordx4 v[156:159], v11, s[24:25] offset:2048 sc1\n\t"
    "global_load_dwordx4 v[160:163], v11, s[24:25] offset:3072 sc1\n\t"
    "s_add_u32 s24, s24, 4096\n\t"
    "s_addc_u32 s25, s25, 0\n\t"
    "global_load_dwordx4 v[164:167], v11, s[24:25] offset:0 sc1\n\t"
    "global_load_dwordx4 v[168:171], v11, s[24:25] offset:1024 sc1\n\t"
    "global_load_dwordx4 v[172:175], v11, s[24:25] offset:2048 sc1\n\t"
    "global_load_dwordx4 v[176:179], v11, s[24:25] offset:3072 sc1\n\t"
    "s_add_u32 s24, s24, 4096\n\t"
    "s_addc_u32 s25, s25, 0\n\t"
    "ds_read_b128 v[60:63], v92 offset:1536\n\t"
    "ds_read_b128 v[64:67], v93 offset:1536\n\t"
    "ds_read_b128 v[68:71], v94 offset:1536\n\t"
    "ds_read_b128 v[72:75], v95 offset:1536\n\t"
    "s_waitcnt lgkmcnt(0)\n\t"
    "v_smfmac_f32_16x16x64_bf16 v[44:47], a[192:195], v[60:67], v36\n\t"
    "v_smfmac_f32_16x16x64_bf16 v[48:51], a[196:199], v[60:67], v36 abid:1\n\t"
    "v_smfmac_f32_16x16x64_bf16 v[52:55], a[200:203], v[60:67], v37\n\t"
    "v_smfmac_f32_16x16x64_bf16 v[56:59], a[204:207], v[60:67], v37 abid:1\n\t"
    "v_smfmac_f32_16x16x64_bf16 v[44:47], a[208:211], v[68:75], v38\n\t"
    "v_smfmac_f32_16x16x64_bf16 v[48:51], a[212:215], v[68:75], v38 abid:1\n\t"
    "v_smfmac_f32_16x16x64_bf16 v[52:55], a[216:219], v[68:75], v39\n\t"
    "v_smfmac_f32_16x16x64_bf16 v[56:59], a[220:223], v[68:75], v39 abid:1\n\t"
    "s_waitcnt vmcnt(0)\n\t"
    "v_smfmac_f32_16x16x64_bf16 v[44:47], v[148:151], v[60:67], v10\n\t"
    "v_smfmac_f32_16x16x64_bf16 v[48:51], v[152:155], v[60:67], v10\n\t"
    "v_smfmac_f32_16x16x64_bf16 v[52:55], v[156:159], v[60:67], v10\n\t"
    "v_smfmac_f32_16x16x64_bf16 v[56:59], v[160:163], v[60:67], v10\n\t"
    "v_smfmac_f32_16x16x64_bf16 v[44:47], v[164:167], v[68:75], v10\n\t"
    "v_smfmac_f32_16x16x64_bf16 v[48:51], v[168:171], v[68:75], v10\n\t"
    "v_smfmac_f32_16x16x64_bf16 v[52:55], v[172:175], v[68:75], v10\n\t"
    "v_smfmac_f32_16x16x64_bf16 v[56:59], v[176:179], v[68:75], v10\n\t"
    "s_nop 3\n\t"
    "global_load_dwordx4 v[148:151], v11, s[24:25] offset:0 sc1\n\t"
    "global_load_dwordx4 v[152:155], v11, s[24:25] offset:1024 sc1\n\t"
    "global_load_dwordx4 v[156:159], v11, s[24:25] offset:2048 sc1\n\t"
    "global_load_dwordx4 v[160:163], v11, s[24:25] offset:3072 sc1\n\t"
    "s_add_u32 s24, s24, 4096\n\t"
    "s_addc_u32 s25, s25, 0\n\t"
    "global_load_dwordx4 v[164:167], v11, s[24:25] offset:0 sc1\n\t"
    "global_load_dwordx4 v[168:171], v11, s[24:25] offset:1024 sc1\n\t"
    "global_load_dwordx4 v[172:175], v11, s[24:25] offset:2048 sc1\n\t"
    "global_load_dwordx4 v[176:179], v11, s[24:25] offset:3072 sc1\n\t"
    "s_add_u32 s24, s24, 4096\n\t"
    "s_addc_u32 s25, s25, 0\n\t"
    "ds_read_b128 v[60:63], v92 offset:1792\n\t"
    "ds_read_b128 v[64:67], v93 offset:1792\n\t"
    "ds_read_b128 v[68:71], v94 offset:1792\n\t"
    "ds_read_b128 v[72:75], v95 offset:1792\n\t"
    "s_waitcnt lgkmcnt(0)\n\t"
    "v_smfmac_f32_16x16x64_bf16 v[44:47], a[224:227], v[60:67], v40\n\t"
    "v_smfmac_f32_16x16x64_bf16 v[48:51], a[228:231], v[60:67], v40 abid:1\n\t"
    "v_smfmac_f32_16x16x64_bf16 v[52:55], a[232:235], v[60:67], v41\n\t"
    "v_smfmac_f32_16x16x64_bf16 v[56:59], a[236:239], v[60:67], v41 abid:1\n\t"
    "v_smfmac_f32_16x16x64_bf16 v[44:47], a[240:243], v[68:75], v42\n\t"
    "v_smfmac_f32_16x16x64_bf16 v[48:51], a[244:247], v[68:75], v42 abid:1\n\t"
    "v_smfmac_f32_16x16x64_bf16 v[52:55], a[248:251], v[68:75], v43\n\t"
    "v_smfmac_f32_16x16x64_bf16 v[56:59], a[252:255], v[68:75], v43 abid:1\n\t"
    "s_waitcnt vmcnt(0)\n\t"
    "v_smfmac_f32_16x16x64_bf16 v[44:47], v[148:151], v[60:67], v10\n\t"
    "v_smfmac_f32_16x16x64_bf16 v[48:51], v[152:155], v[60:67], v10\n\t"
    "v_smfmac_f32_16x16x64_bf16 v[52:55], v[156:159], v[60:67], v10\n\t"
    "v_smfmac_f32_16x16x64_bf16 v[56:59], v[160:163], v[60:67], v10\n\t"
    "v_smfmac_f32_16x16x64_bf16 v[44:47], v[164:167], v[68:75], v10\n\t"
    "v_smfmac_f32_16x16x64_bf16 v[48:51], v[168:171], v[68:75], v10\n\t"
    "v_smfmac_f32_16x16x64_bf16 v[52:55], v[172:175], v[68:75], v10\n\t"
    "v_smfmac_f32_16x16x64_bf16 v[56:59], v[176:179], v[68:75], v10\n\t"
    "s_nop 3\n\t"
    "s_nop 7\n\t"
    "v_cvt_pk_bf16_f32 v236, v44, v45\n\t"
    "v_cvt_pk_bf16_f32 v237, v46, v47\n\t"
    "v_cvt_pk_bf16_f32 v238, v48, v49\n\t"
    "v_cvt_pk_bf16_f32 v239, v50, v51\n\t"
    "v_cvt_pk_bf16_f32 v240, v52, v53\n\t"
    "v_cvt_pk_bf16_f32 v241, v54, v55\n\t"
    "v_cvt_pk_bf16_f32 v242, v56, v57\n\t"
    "v_cvt_pk_bf16_f32 v243, v58, v59\n\t"
    "s_nop 1\n\t"
    "v_mov_b32_dpp v244, v240 row_ror:8 row_mask:0xf bank_mask:0xf\n\t"
    "v_mov_b32_dpp v245, v241 row_ror:8 row_mask:0xf bank_mask:0xf\n\t"
    "v_mov_b32_dpp v246, v242 row_ror:8 row_mask:0xf bank_mask:0xf\n\t"
    "v_mov_b32_dpp v247, v243 row_ror:8 row_mask:0xf bank_mask:0xf\n\t"
    "v_mov_b32_dpp v240, v236 row_ror:8 row_mask:0xf bank_mask:0x3\n\t"
    "v_mov_b32_dpp v241, v237 row_ror:8 row_mask:0xf bank_mask:0x3\n\t"
    "v_mov_b32_dpp v242, v238 row_ror:8 row_mask:0xf bank_mask:0x3\n\t"
    "v_mov_b32_dpp v243, v239 row_ror:8 row_mask:0xf bank_mask:0x3\n\t"
    "v_mov_b32_dpp v236, v244 quad_perm:[0,1,2,3] row_mask:0xf bank_mask:0xc\n\t"
    "v_mov_b32_dpp v237, v245 quad_perm:[0,1,2,3] row_mask:0xf bank_mask:0xc\n\t"
    "v_mov_b32_dpp v238, v246 quad_perm:[0,1,2,3] row_mask:0xf bank_mask:0xc\n\t"
    "v_mov_b32_dpp v239, v247 quad_perm:[0,1,2,3] row_mask:0xf bank_mask:0xc\n\t"
    "buffer_store_dwordx4 v[236:239], v0, s[48:51], s55 offen sc1\n\t"
    "buffer_store_dwordx4 v[240:243], v1, s[48:51], s55 offen sc1\n\t"
    "s_add_u32 s54, s54, 32768\n\t"
    "s_xor_b32 s55, s54, s53\n\t"
    "s_add_u32 s22, s22, 2048\n\t"
    "s_cmp_eq_u32 s22, 10240\n\t"
    "s_cselect_b32 s22, 0, s22\n\t"
    "s_waitcnt vmcnt(0) lgkmcnt(0)\n\t"
    "s_cmp_lt_u32 s18, 2\n\t"
    "s_cbranch_scc1 .LCnobar_%=\n\t"
    "s_barrier\n\t"
    ".LCnobar_%=:\n\t"
    "s_add_u32 s18, s18, 1\n\t"
    "s_cmp_lg_u32 s18, 16\n\t"
    "s_cbranch_scc1 .LloopC_%=\n\t"
    "s_mov_b32 s47, 0\n\t"
    ".Lend_%=:\n\t"
    "s_or_b32 s26, %3, %4\n\t"
    "s_cmp_lg_u32 s26, 0\n\t"
    "s_cbranch_scc1 .Lfin_%=\n\t"
    "s_mov_b64 s[44:45], %2\n\t"
    "s_add_u32 s44, s44, 0x1000000\n\t"
    "s_addc_u32 s45, s45, 0\n\t"
    "s_mov_b32 s46, 0x10000\n\t"
    "s_cmp_eq_u32 s47, 1\n\t"
    "s_cbranch_scc1 .Lchk_%=\n\t"
    ".Lall_%=:\n\t"
    "global_load_dwordx4 v[68:71], v11, s[44:45] offset:0 sc1\n\t"
    "global_load_dwordx4 v[72:75], v11, s[44:45] offset:1024 sc1\n\t"
    "global_load_dwordx4 v[76:79], v11, s[44:45] offset:2048 sc1\n\t"
    "global_load_dwordx4 v[80:83], v11, s[44:45] offset:3072 sc1\n\t"
    "s_waitcnt vmcnt(0)\n\t"
    ".Lchk_%=:\n\t"
    "s_waitcnt vmcnt(2)\n\t"
    "v_xor_b32 v69, %6, v69\n\t"
    "v_xor_b32 v71, %6, v71\n\t"
    "v_xor_b32 v73, %6, v73\n\t"
    "v_xor_b32 v75, %6, v75\n\t"
    "v_xor_b32 v77, %6, v77\n\t"
    "v_xor_b32 v79, %6, v79\n\t"
    "v_xor_b32 v81, %6, v81\n\t"
    "v_xor_b32 v83, %6, v83\n\t"
    "v_or3_b32 v69, v69, v71, v73\n\t"
    "v_or3_b32 v75, v75, v77, v79\n\t"
    "v_or3_b32 v69, v69, v81, v83\n\t"
    "v_or_b32 v69, v69, v75\n\t"
    "v_cmp_ne_u32 vcc, 0, v69\n\t"
    "s_cbranch_vccz .Lallok_%=\n\t"
    "s_sleep 8\n\t"
    "s_sub_u32 s46, s46, 1\n\t"
    "s_cmp_lg_u32 s46, 0\n\t"
    "s_cbranch_scc1 .Lall_%=\n\t"
    ".Lallok_%=:\n\t"
    "v_mov_b32 v68, %6\n\t"
    "v_mov_b32 v69, 0x1000\n\t"
    "s_mov_b64 exec, 1\n\t"
    "global_store_dword v69, v68, s[44:45] sc1\n\t"
    "s_mov_b64 exec, -1\n\t"
    ".Lfin_%=:\n\t"
    :: "s"(x), "s"(out), "s"(ws), "s"(blockIdx.x), "s"(wave), "s"(bias), "s"(tag)
    : "v0","v1","v2","v3","v4","v5","v6","v7","v8","v9","v10","v11","v12","v13","v14","v15","v16","v17","v18","v19","v20","v21","v22","v23","v24","v25","v26","v27","v28","v29","v30","v31","v32","v33","v34","v35","v36","v37","v38","v39","v40","v41","v42","v43","v44","v45","v46","v47","v48","v49","v50","v51","v52","v53","v54","v55","v56","v57","v58","v59","v60","v61","v62","v63","v64","v65","v66","v67","v68","v69","v70","v71","v72","v73","v74","v75","v76","v77","v78","v79","v80","v81","v82","v83","v84","v85","v86","v87","v88","v89","v90","v91","v92","v93","v94","v95","v96","v97","v98","v99","v100","v101","v102","v103","v104","v105","v106","v107","v108","v109","v110","v111","v112","v113","v114","v115","v116","v117","v118","v119","v120","v121","v122","v123","v124","v125","v126","v127","v128","v129","v130","v131","v132","v133","v134","v135","v136","v137","v138","v139","v140","v141","v142","v143","v144","v145","v146","v147","v148","v149","v150","v151","v152","v153","v154","v155","v156","v157","v158","v159","v160","v161","v162","v163","v164","v165","v166","v167","v168","v169","v170","v171","v172","v173","v174","v175","v176","v177","v178","v179","v180","v181","v182","v183","v184","v185","v186","v187","v188","v189","v190","v191","v192","v193","v194","v195","v196","v197","v198","v199","v200","v201","v202","v203","v204","v205","v206","v207","v208","v209","v210","v211","v212","v213","v214","v215","v216","v217","v218","v219","v220","v221","v222","v223","v224","v225","v226","v227","v228","v229","v230","v231","v232","v233","v234","v235","v236","v237","v238","v239","v240","v241","v242","v243","v244","v245","v246","v247","v248","v249","v250","v251","v252","v253","v254","v255","a0","a1","a2","a3","a4","a5","a6","a7","a8","a9","a10","a11","a12","a13","a14","a15","a16","a17","a18","a19","a20","a21","a22","a23","a24","a25","a26","a27","a28","a29","a30","a31","a32","a33","a34","a35","a36","a37","a38","a39","a40","a41","a42","a43","a44","a45","a46","a47","a48","a49","a50","a51","a52","a53","a54","a55","a56","a57","a58","a59","a60","a61","a62","a63","a64","a65","a66","a67","a68","a69","a70","a71","a72","a73","a74","a75","a76","a77","a78","a79","a80","a81","a82","a83","a84","a85","a86","a87","a88","a89","a90","a91","a92","a93","a94","a95","a96","a97","a98","a99","a100","a101","a102","a103","a104","a105","a106","a107","a108","a109","a110","a111","a112","a113","a114","a115","a116","a117","a118","a119","a120","a121","a122","a123","a124","a125","a126","a127","a128","a129","a130","a131","a132","a133","a134","a135","a136","a137","a138","a139","a140","a141","a142","a143","a144","a145","a146","a147","a148","a149","a150","a151","a152","a153","a154","a155","a156","a157","a158","a159","a160","a161","a162","a163","a164","a165","a166","a167","a168","a169","a170","a171","a172","a173","a174","a175","a176","a177","a178","a179","a180","a181","a182","a183","a184","a185","a186","a187","a188","a189","a190","a191","a192","a193","a194","a195","a196","a197","a198","a199","a200","a201","a202","a203","a204","a205","a206","a207","a208","a209","a210","a211","a212","a213","a214","a215","a216","a217","a218","a219","a220","a221","a222","a223","a224","a225","a226","a227","a228","a229","a230","a231","a232","a233","a234","a235","a236","a237","a238","a239","a240","a241","a242","a243","a244","a245","a246","a247","a248","a249","a250","a251","a252","a253","a254","a255","s8","s9","s10","s11","s12","s13","s14","s15","s16","s17","s18","s19","s20","s21","s22","s23","s24","s25","s26","s27","s28","s29","s30","s31","s34","s35","s36","s37","s38","s39","s40","s41","s42","s43","s44","s45","s46","s47","s48","s49","s50","s51","s52","s53","s54","s55","s56","s57","s58","s59","vcc","scc","memory");
}

extern "C" void kernel_launch(void* const* d_in, const int* in_sizes, int n_in, void* d_out, int out_size, void* d_ws,
                              size_t ws_size, hipStream_t stream) {
  sp24_fused<<<256, 256, 0, stream>>>(d_in[0], d_out, (unsigned char*)d_ws, d_in[2], (const unsigned short*)d_in[1]);
}
```
